```python
import torch

def _softmax(x, axis=-1):
    return torch.softmax(x, dim=axis)
def _gelu(x):
    return torch.nn.functional.gelu(x, approximate="tanh")
def _silu(x):
    return torch.nn.functional.silu(x)
def _sigmoid(x):
    return torch.sigmoid(x)
def _layernorm(x, axis=-1, eps=1e-5):
    m = x.mean(dim=axis, keepdim=True)
    v = x.var(dim=axis, keepdim=True, unbiased=False)
    return (x - m) / torch.sqrt(v + eps)
def _scatter_add(tgt, idx, src, axis=0):
    return tgt.clone().index_add_(axis, idx, src)
def _segment_sum(src, seg, n):
    out = torch.zeros((n,) + src.shape[1:], dtype=src.dtype)
    return out.index_add_(0, seg, src)
def _pad(x, pw):
    flat = tuple(v for pair in reversed(pw) for v in pair)
    return torch.nn.functional.pad(x, flat)

kernel_name = 'gated_mix__sliding_block_cached__dense_activation_squared_relu__decode'

def setup_inputs():

    _w = (lambda a: (lambda t: t.to(torch.bfloat16) if t.is_floating_point() else t)(torch.as_tensor(a)))
    def _A_setup():
        B, Sq, D = 2, 1, 576
        Hq, Hkv, Sk, Dh = 32, 16, 32768, 128
        assert Hq % Hkv == 0, (Hq, Hkv)
        return {
            "x": _w((lambda *s: torch.randn(*s, dtype=torch.bfloat16) * 0.05)(B, Sq, D)),
            "k_cache": _w((lambda *s: torch.randn(*s, dtype=torch.bfloat16) * 6.4)(B, Hkv, Sk, Dh)),
            "v_cache": _w((lambda *s: torch.randn(*s, dtype=torch.bfloat16) * 0.4)(B, Hkv, Sk, Dh)),
            "wq": _w((lambda *s: torch.randn(*s, dtype=torch.bfloat16) * 6.4)(D, Hq * Dh) * (D ** -0.5)),
            "wo": _w((lambda *s: torch.randn(*s, dtype=torch.bfloat16) * 0.4)(Hq * Dh, D) * ((Hq * Dh) ** -0.5)),
        }
    _A_in = _A_setup()

    def _B_setup():
        B, S, D, F = 2, 1, 576, 4608
        return {
            "x": _w((lambda *s: torch.randn(*s, dtype=torch.bfloat16) * 0.05)(B, S, D)),
            "w1": _w((lambda *s: torch.randn(*s, dtype=torch.bfloat16) * 0.05)(D, F)),
            "b1": _w((lambda *s: torch.randn(*s, dtype=torch.bfloat16) * 0.05)(F)),
            "w2": _w((lambda *s: torch.randn(*s, dtype=torch.bfloat16) * 0.05)(F, D)),
            "b2": _w((lambda *s: torch.randn(*s, dtype=torch.bfloat16) * 0.05)(D)),
        }
    _B_in = _B_setup()

    return {
        "A_x": _A_in["x"],
        "A_k_cache": _A_in["k_cache"],
        "A_v_cache": _A_in["v_cache"],
        "A_wq": _A_in["wq"],
        "A_wo": _A_in["wo"],
        "B_x": _B_in["x"],
        "B_w1": _B_in["w1"],
        "B_b1": _B_in["b1"],
        "B_w2": _B_in["w2"],
        "B_b2": _B_in["b2"],
        "gate_w": _w((lambda *s: torch.randn(*s, dtype=torch.bfloat16) * 0.05)(576) * 16.0),
    }

def reference(A_x, A_k_cache, A_v_cache, A_wq, A_wo, B_x, B_w1, B_b1, B_w2, B_b2, gate_w):
    def _A_ref(x, k_cache, v_cache, wq, wo):
        B, Sq, D = x.shape
        Hq, Dh = 32, 128
        Hkv, Sk = k_cache.shape[1], k_cache.shape[2]
        W = min(4096, max(256, Sk // 4))
        rep = Hq // Hkv
        q = (lambda x, *d: x.permute(*d))((x @ wq).reshape(B, Sq, Hq, Dh), 0, 2, 1, 3)
        k = (lambda x, n, axis: torch.repeat_interleave(x, n, dim=axis))(k_cache[:, :, Sk - W:, :], rep, axis=1)
        v = (lambda x, n, axis: torch.repeat_interleave(x, n, dim=axis))(v_cache[:, :, Sk - W:, :], rep, axis=1)
        scores = torch.einsum("bhqd,bhkd->bhqk", q, k) * (Dh ** -0.5)
        p = _softmax(scores, axis=-1)
        o = torch.einsum("bhqk,bhkd->bhqd", p, v)
        return (lambda x, *d: x.permute(*d))(o, 0, 2, 1, 3).reshape(B, Sq, Hq * Dh) @ wo

    def _B_ref(x, w1, b1, w2, b2):
        h = torch.einsum("bsd,df->bsf", x, w1) + b1
        zero = torch.zeros_like(h)
        r = torch.where(h > zero, h, zero)
        h = r * r
        out = torch.einsum("bsf,fd->bsd", h, w2) + b2
        return out

    x = A_x
    g = _sigmoid((x * gate_w).sum(-1, keepdims=True))
    a = _A_ref(x, A_k_cache, A_v_cache, A_wq, A_wo)
    b = _B_ref(x, B_w1, B_b1, B_w2, B_b2)
    return g * a + (1 - g) * b
```

```cpp
#include <hip/hip_runtime.h>
#include <hip/hip_bf16.h>
#include <stdint.h>

constexpr int B = 2, D = 576, HQ = 32, HKV = 16, SK = 32768, DH = 128, W = 4096, F = 4608;
constexpr int NSPLIT = 8, KPS = W / NSPLIT;
typedef unsigned short u16;
__device__ __forceinline__ float bf2f(u16 v) { return __uint_as_float((unsigned)v << 16); }
__device__ __forceinline__ u16 f2bf(float f) { unsigned u = __float_as_uint(f); u += 0x7FFFu + ((u >> 16) & 1u); return (u16)(u >> 16); }
__device__ __forceinline__ float rbf(float f) { return bf2f(f2bf(f)); }

__global__ void k1(const u16* ax, const u16* wq, const u16* bx, const u16* w1, const u16* b1, const u16* gw, float* q, float* h, float* g) {
  int col = blockIdx.x * 64 + threadIdx.x;
  __shared__ float xs[2][2][D];
  for (int i = threadIdx.x; i < 2 * D; i += 64) { xs[0][i / D][i % D] = bf2f(ax[i]); xs[1][i / D][i % D] = bf2f(bx[i]); }
  __syncthreads();
  if (col < HQ * DH) {
    float a0 = 0, a1 = 0;
    for (int k = 0; k < D; ++k) { float w = bf2f(wq[(size_t)k * (HQ * DH) + col]); a0 += xs[0][0][k] * w; a1 += xs[0][1][k] * w; }
    q[col] = rbf(a0); q[HQ * DH + col] = rbf(a1);
  } else {
    int c = col - HQ * DH;
    float a0 = 0, a1 = 0;
    for (int k = 0; k < D; ++k) { float w = bf2f(w1[(size_t)k * F + c]); a0 += xs[1][0][k] * w; a1 += xs[1][1][k] * w; }
    float bb = bf2f(b1[c]);
    a0 = rbf(rbf(a0) + bb); a1 = rbf(rbf(a1) + bb);
    a0 = a0 > 0 ? a0 : 0; a1 = a1 > 0 ? a1 : 0;
    h[c] = rbf(a0 * a0); h[F + c] = rbf(a1 * a1);
  }
  if (blockIdx.x == 0 && threadIdx.x < 2) {
    float s = 0;
    for (int k = 0; k < D; ++k) s += rbf(xs[0][threadIdx.x][k] * bf2f(gw[k]));
    s = rbf(s);
    g[threadIdx.x] = rbf(1.f / (1.f + __expf(-s)));
  }
}

__global__ void k2(const u16* kc, const u16* vc, const float* q, float* pm, float* pl, float* po) {
  int s = blockIdx.x, kvh = blockIdx.y, b = blockIdx.z, tid = threadIdx.x;
  __shared__ float qs[2][DH]; __shared__ float sc[2][KPS]; __shared__ float red[2][256];
  for (int i = tid; i < 2 * DH; i += 256) qs[i / DH][i % DH] = q[b * HQ * DH + (kvh * 2 + i / DH) * DH + i % DH];
  __syncthreads();
  const u16* kb = kc + ((size_t)(b * HKV + kvh) * SK + (SK - W) + s * KPS) * DH;
  const u16* vb = vc + ((size_t)(b * HKV + kvh) * SK + (SK - W) + s * KPS) * DH;
  for (int k = tid; k < KPS; k += 256) {
    float a0 = 0, a1 = 0;
    for (int d = 0; d < DH; ++d) { float kv = bf2f(kb[(size_t)k * DH + d]); a0 += qs[0][d] * kv; a1 += qs[1][d] * kv; }
    sc[0][k] = a0 * 0.08838834764831845f; sc[1][k] = a1 * 0.08838834764831845f;
  }
  __syncthreads();
  float m[2], l[2];
  for (int hh = 0; hh < 2; ++hh) {
    float mm = -INFINITY; for (int k = 0; k < KPS; ++k) mm = fmaxf(mm, sc[hh][k]);
    m[hh] = mm;
  }
  __syncthreads();
  for (int k = tid; k < KPS; k += 256) { sc[0][k] = __expf(sc[0][k] - m[0]); sc[1][k] = __expf(sc[1][k] - m[1]); }
  __syncthreads();
  for (int hh = 0; hh < 2; ++hh) { float ss = 0; for (int k = 0; k < KPS; ++k) ss += sc[hh][k]; l[hh] = ss; }
  int hh = tid / DH, d = tid % DH;
  float acc = 0;
  for (int k = 0; k < KPS; ++k) acc += sc[hh][k] * bf2f(vb[(size_t)k * DH + d]);
  int row = (b * HQ + kvh * 2 + hh);
  po[((size_t)row * NSPLIT + s) * DH + d] = acc;
  if (d == 0) { pm[row * NSPLIT + s] = m[hh]; pl[row * NSPLIT + s] = l[hh]; }
}

__global__ void k3(const float* pm, const float* pl, const float* po, float* o) {
  int row = blockIdx.x, d = threadIdx.x;
  float M = -INFINITY; for (int s = 0; s < NSPLIT; ++s) M = fmaxf(M, pm[row * NSPLIT + s]);
  float L = 0, acc = 0;
  for (int s = 0; s < NSPLIT; ++s) { float w = __expf(pm[row * NSPLIT + s] - M); L += w * pl[row * NSPLIT + s]; acc += w * po[((size_t)row * NSPLIT + s) * DH + d]; }
  o[row * DH + d] = rbf(acc / L);
}

__global__ void k4(const float* o, const float* h, const float* g, const u16* wo, const u16* w2, const u16* b2, u16* out) {
  int c = blockIdx.x * 64 + threadIdx.x, b = blockIdx.y, y = threadIdx.y;
  float a = 0, m = 0;
  for (int k = y; k < HQ * DH; k += 8) a += o[b * HQ * DH + k] * bf2f(wo[(size_t)k * D + c]);
  for (int k = y; k < F; k += 8) m += h[b * F + k] * bf2f(w2[(size_t)k * D + c]);
  __shared__ float ra[8][64], rm[8][64];
  ra[y][threadIdx.x] = a; rm[y][threadIdx.x] = m;
  __syncthreads();
  if (y == 0) {
    a = 0; m = 0; for (int i = 0; i < 8; ++i) { a += ra[i][threadIdx.x]; m += rm[i][threadIdx.x]; }
    a = rbf(a); m = rbf(rbf(m) + bf2f(b2[c]));
    float gg = g[b];
    out[b * D + c] = f2bf(rbf(gg * a) + rbf(rbf(1.f - gg) * m));
  }
}

extern "C" void kernel_launch(void* const* d_in, const int* in_sizes, int n_in, void* d_out, int out_size, void* d_ws, size_t ws_size, hipStream_t stream) {
  const u16* ax = (const u16*)d_in[0]; const u16* kc = (const u16*)d_in[1]; const u16* vc = (const u16*)d_in[2];
  const u16* wq = (const u16*)d_in[3]; const u16* wo = (const u16*)d_in[4]; const u16* bx = (const u16*)d_in[0];
  const u16* w1 = (const u16*)d_in[6]; const u16* b1 = (const u16*)d_in[7]; const u16* w2 = (const u16*)d_in[8];
  const u16* b2 = (const u16*)d_in[9]; const u16* gw = (const u16*)d_in[10];
  float* ws = (float*)d_ws;
  float* q = ws; float* h = q + 2 * 4096; float* g = h + 2 * F; float* pm = g + 64; float* pl = pm + 64 * NSPLIT; float* po = pl + 64 * NSPLIT; float* o = po + 64 * NSPLIT * DH;
  k1<<<(4096 + F) / 64, 64, 0, stream>>>(ax, wq, bx, w1, b1, gw, q, h, g);
  k2<<<dim3(NSPLIT, HKV, B), 256, 0, stream>>>(kc, vc, q, pm, pl, po);
  k3<<<64, DH, 0, stream>>>(pm, pl, po, o);
  k4<<<dim3(D / 64, B), dim3(64, 8), 0, stream>>>(o, h, g, wo, w2, b2, (u16*)d_out);
}
```

```cpp
#include <hip/hip_runtime.h>
#include <stdint.h>

typedef unsigned u32; typedef unsigned long long u64; typedef unsigned short u16;
typedef u32 v4u __attribute__((ext_vector_type(4)));
typedef u32 v2u __attribute__((ext_vector_type(2)));
typedef float f32x4 __attribute__((ext_vector_type(4)));
typedef float f32x2 __attribute__((ext_vector_type(2)));
typedef short s16x4 __attribute__((ext_vector_type(4)));
typedef short s16x8 __attribute__((ext_vector_type(8)));
typedef __bf16 bf16x8 __attribute__((ext_vector_type(8)));
typedef __bf16 bf16x2 __attribute__((ext_vector_type(2)));
#define GAS __attribute__((address_space(1)))
#define LAS __attribute__((address_space(3)))

#define ABL_NOW1 0
#define ABL_NOQ 0
#define ABL_NOH 0
#define KDELAY 0
#define STAMPS 0

constexpr int D = 576, NQ = 4096, F = 4608, SK = 32768, WIN = 4096;
constexpr int NWG = 256, NTHR = 576;
constexpr u32 QP_OFF = 4096, HP_OFF = QP_OFF + 2 * 4 * NQ * 8, ST_OFF = HP_OFF + 3 * F * 16, H2_OFF = ST_OFF + 16 * 16 * 4 * 8,
              H3_OFF = H2_OFF + 16 * 16 * 256 * 8, WS_USED = H3_OFF + 144 * 64 * 4 * 16;
constexpr int L_K = 0, L_RED = 0, L_WI = 8192, L_WT = 131072  , L_XS = L_WT + 192 * 128, L_QS = L_XS + 2 * D * 2,
              L_WST = L_QS + 512, L_GF = L_WST + 128, L_HC = L_GF + 64, L_OC = L_HC + 2 * 80 * 2, L_FT = L_OC + 2 * 64 * 2, L_G = L_FT + 64,
              L_SY = L_G + 32, L_END = L_SY + 64;
static_assert(L_END <= 163840 && L_WI + 136 * 288 <= 131072 && (L_HC % 16) == 0 && (L_OC % 16) == 0 && (L_QS % 16) == 0, "LDS map");
enum { SY_Q = 0, SY_C0, SY_G, SY_H, SY_C1, SY_C2, SY_F, SY_C3, SY_W, SY_QP, SY_W1 };
constexpr int OOB = 0x40000000;
constexpr u32 CAP_LDS = 1u << 20, CAP_GLB = 1u << 16;
constexpr float C2 = 0.08838834764831845f * 1.4426950408889634f;

__device__ __forceinline__ float asf(u32 x) { return __builtin_bit_cast(float, x); }
__device__ __forceinline__ u32 asu(float x) { return __builtin_bit_cast(u32, x); }
__device__ __forceinline__ u32 bf16r(float f) { u32 u = asu(f); return (u + 0x7FFFu + ((u >> 16) & 1u)) >> 16; }
__device__ __forceinline__ u32 pk_bf16(float lo, float hi) { return bf16r(lo) | (bf16r(hi) << 16); }
__device__ __forceinline__ float dot2(u32 a, u32 b, float c) { return __builtin_amdgcn_fdot2_f32_bf16(__builtin_bit_cast(bf16x2, a), __builtin_bit_cast(bf16x2, b), c, false); }
template <int CTRL> __device__ __forceinline__ float dpp(float x) { return asf((u32)__builtin_amdgcn_mov_dpp((int)asu(x), CTRL, 0xf, 0xf, true)); }
constexpr int XOR1 = 0xB1, XOR2 = 0x4E, HMIR = 0x141, ROR8 = 0x128, ROR4 = 0x124, ROR2 = 0x122, ROR1 = 0x121;
__device__ __forceinline__ float sum16(float x) { auto s = __builtin_amdgcn_permlane16_swap(asu(x), asu(x), false, false); return asf(s[0]) + asf(s[1]); }
__device__ __forceinline__ float sum32(float x) { auto t = __builtin_amdgcn_permlane32_swap(asu(x), asu(x), false, false); return asf(t[0]) + asf(t[1]); }
__device__ __forceinline__ float sum16_32(float x) { return sum32(sum16(x)); }
__device__ __forceinline__ float max16_32(float x) {
  auto s = __builtin_amdgcn_permlane16_swap(asu(x), asu(x), false, false); x = fmaxf(asf(s[0]), asf(s[1]));
  auto t = __builtin_amdgcn_permlane32_swap(asu(x), asu(x), false, false); return fmaxf(asf(t[0]), asf(t[1]));
}
__device__ __forceinline__ float wave_sum(float x) { x += dpp<ROR1>(x); x += dpp<ROR2>(x); x += dpp<ROR4>(x); x += dpp<ROR8>(x); return sum16_32(x); }
__device__ __forceinline__ float wave_max(float x) { x = fmaxf(x, dpp<ROR1>(x)); x = fmaxf(x, dpp<ROR2>(x)); x = fmaxf(x, dpp<ROR4>(x)); x = fmaxf(x, dpp<ROR8>(x)); return max16_32(x); }
__device__ __forceinline__ u32 g_lo(float v, u32 tl) { return ((asu(v) + 0x80u) >> 8) | tl; }
__device__ __forceinline__ float g_val(u32 lo) { return asf(lo << 8); }
__device__ __forceinline__ u32 g_bad(v4u q, u32 tl, u32 th) { return (((q.x ^ tl) | (q.z ^ tl)) & 0xFF000000u) | (q.y ^ th) | (q.w ^ th); }
__device__ __forceinline__ v4u g_pack(float a, float b, u32 tl, u32 th) { return v4u{g_lo(a, tl), th, g_lo(b, tl), th}; }
#define WAITV(n) asm volatile("s_waitcnt vmcnt(" #n ")" ::: "memory")
#define WAITL() asm volatile("s_waitcnt lgkmcnt(0)" ::: "memory")
#define MEMBAR() asm volatile("" ::: "memory")
#define SB() __builtin_amdgcn_sched_barrier(0)
__device__ __forceinline__ void spin_lds(LAS u32* p, u32 want) {
#pragma nounroll
  for (u32 n = 0; n < CAP_LDS; ++n) {
    if ((u32)__builtin_amdgcn_readfirstlane((int)*(volatile LAS u32*)p) >= want) break;
    __builtin_amdgcn_s_sleep(1);
  }
  MEMBAR();
}
__device__ __forceinline__ void spin_lds_tight(LAS u32* p, u32 want) {
#pragma nounroll
  for (u32 n = 0; n < CAP_LDS; ++n) { if ((u32)__builtin_amdgcn_readfirstlane((int)*(volatile LAS u32*)p) >= want) break; }
  MEMBAR();
}
__device__ __forceinline__ void lds_arrive(LAS u32* p, int lane) { MEMBAR(); if (lane == 0) __hip_atomic_fetch_add(p, 1u, __ATOMIC_RELAXED, __HIP_MEMORY_SCOPE_WORKGROUP); MEMBAR(); }
__device__ __forceinline__ void lds_set(LAS u32* p, int lane) { MEMBAR(); if (lane == 0) *(volatile LAS u32*)p = 1u; MEMBAR(); }
__device__ __forceinline__ s16x4 trd(LAS unsigned char* p) { return __builtin_amdgcn_ds_read_tr16_b64_v4i16((LAS s16x4*)p); }
__device__ __forceinline__ f32x4 mfma32v(v4u a, v4u b, f32x4 c) { return __builtin_amdgcn_mfma_f32_16x16x32_bf16(__builtin_bit_cast(bf16x8, a), __builtin_bit_cast(bf16x8, b), c, 0, 0, 0); }
__device__ __forceinline__ f32x4 mfma32(v4u a, s16x4 b0, s16x4 b1, f32x4 c) {
  s16x8 b = __builtin_shufflevector(b0, b1, 0, 1, 2, 3, 4, 5, 6, 7);
  return __builtin_amdgcn_mfma_f32_16x16x32_bf16(__builtin_bit_cast(bf16x8, a), __builtin_bit_cast(bf16x8, b), c, 0, 0, 0);
}
__device__ __forceinline__ f32x4 mfma16(s16x4 a, s16x4 b, f32x4 c) { return __builtin_amdgcn_mfma_f32_16x16x16bf16_1k(a, b, c, 0, 0, 0); }
__device__ __forceinline__ void glds_nt(const void* gsrc, u32 lds_dst) {
  u32 keep;
  asm volatile("s_mov_b32 %0, m0\n\ts_mov_b32 m0, %2\n\ts_nop 0\n\tglobal_load_lds_dwordx4 %1, off nt\n\ts_mov_b32 m0, %0" : "=&s"(keep) : "v"(gsrc), "s"(lds_dst) : "memory");
}
__device__ __forceinline__ void glds_nt_s(const void* sbase, u32 voff, u32 lds_dst) {
  u32 keep;
  asm volatile("s_mov_b32 %0, m0\n\ts_mov_b32 m0, %3\n\ts_nop 0\n\tglobal_load_lds_dwordx4 %1, %2 nt\n\ts_mov_b32 m0, %0" : "=&s"(keep) : "v"(voff), "s"(sbase), "s"(lds_dst) : "memory");
}
__device__ __forceinline__ void glds(const void* gsrc, u32 lds_dst) {
  u32 keep;
  asm volatile("s_mov_b32 %0, m0\n\ts_mov_b32 m0, %2\n\ts_nop 0\n\tglobal_load_lds_dwordx4 %1, off\n\ts_mov_b32 m0, %0" : "=&s"(keep) : "v"(gsrc), "s"(lds_dst) : "memory");
}
__device__ __forceinline__ v4u ldnt(const u16* base, u32 off) { return __builtin_nontemporal_load((const GAS v4u*)((const GAS char*)base + off)); }
__device__ __forceinline__ v4u ldg(__amdgpu_buffer_rsrc_t rs, int off) { return __builtin_amdgcn_raw_buffer_load_b128(rs, off, 0, 16); }
__device__ __forceinline__ void stg(v4u v, __amdgpu_buffer_rsrc_t rs, int off) { __builtin_amdgcn_raw_buffer_store_b128(v, rs, off, 0, 16); }
template <int N, int SLP> __device__ __forceinline__ void gather(__amdgpu_buffer_rsrc_t rs, const int (&off)[N], v4u (&v)[N], u32 tl, u32 th) {
  u32 need = (1u << N) - 1u;
#pragma unroll
  for (int n = 0; n < N; ++n) v[n] = v4u{0, 0, 0, 0};
#pragma nounroll
  for (u32 it = 0; it < CAP_GLB; ++it) {
    v4u t[N];
#pragma unroll
    for (int n = 0; n < N; ++n) t[n] = ldg(rs, ((need >> n) & 1u) ? off[n] : OOB);
#pragma unroll
    for (int n = 0; n < N; ++n) if (((need >> n) & 1u) && g_bad(t[n], tl, th) == 0u) { v[n] = t[n]; need &= ~(1u << n); }
    if (__all(need == 0u)) break;
    MEMBAR();
    if (SLP) __builtin_amdgcn_s_sleep(SLP);
  }
}
#define STAMP(i) do {} while (0)
#define STAMP_DECL do {} while (0)
#define STAMP_FLUSH(base) do {} while (0)

__global__ void __launch_bounds__(NTHR, 3) fused_decode(const u16* x, const u16* kc, const u16* vc, const u16* wq, const u16* wo, const u16* w1, const u16* b1,
    const u16* w2, const u16* b2, const u16* gw, u16* out, unsigned char* ws) {
  extern __shared__ __attribute__((aligned(16))) unsigned char lds_raw[];
  LAS unsigned char* L = (LAS unsigned char*)lds_raw;
  const u32 LB = (u32)(uintptr_t)L;
  LAS u32* SY = (LAS u32*)(L + L_SY);
  asm volatile("" :: "s"(x), "s"(kc), "s"(vc), "s"(wq), "s"(wo), "s"(w1), "s"(b1), "s"(w2), "s"(b2), "s"(gw), "s"(out), "s"(ws));
  const int w = blockIdx.x, lane = threadIdx.x & 63, wave = __builtin_amdgcn_readfirstlane((int)(threadIdx.x >> 6));
  const int bh = w >> 3, s = w & 7, b = bh >> 4, kvh = bh & 15, m = b * 8 + s;
  const int phi = (w & 7) * 32 + (w >> 3);
  const int cbk = phi & 63, rb = phi >> 6;
  const int cb1 = w % 72, rb1 = w / 72;
  const int rho = phi >> 2, cb = phi & 3;
  const int g = lane >> 4, c = lane & 15;
  const __amdgpu_buffer_rsrc_t rs = __builtin_amdgcn_make_buffer_rsrc(ws, 0, WS_USED, 0x00020000);
  {
    const int r0 = 16 * wave + (lane >> 3);
    const u16* src = wq + (size_t)(144 * rb + r0) * NQ + 64 * cbk;
    const int sw0 = (r0 & 2) | ((r0 >> 1) & 4), sw1 = ((r0 + 8) & 2) | (((r0 + 8) >> 1) & 4);
    glds_nt(src + ((lane & 7) ^ sw0) * 8, LB + L_WT + wave * 2048);
    glds_nt(src + 8 * NQ + ((lane & 7) ^ sw1) * 8, LB + L_WT + wave * 2048 + 1024);
  }

  if (wave == 0) {
    __builtin_amdgcn_s_setprio(3);
    STAMP_DECL;
    if (lane < 16) ((volatile LAS u32*)SY)[lane] = 0u;
    u64 E;
    asm volatile("global_load_dwordx2 %0, %1, off sc1" : "=v"(E) : "v"(ws) : "memory");
    glds(x + lane * 8, LB + L_XS);
    glds(x + 512 + lane * 8, LB + L_XS + 1024);
    if (lane < 16) glds(x + 1024 + lane * 8, LB + L_XS + 2048);
    WAITL();
    __builtin_amdgcn_s_barrier();
    asm volatile("s_waitcnt vmcnt(0)" : "+v"(E) :: "memory");
    const u64 T = (E + 1ull) & 0xFFFFFFFFFFull;
    const u32 tl = (u32)__builtin_amdgcn_readfirstlane((int)((u32)(T & 0xFFu) << 24)), th = (u32)__builtin_amdgcn_readfirstlane((int)(u32)(T >> 8));
    STAMP(1);
    spin_lds_tight(SY + SY_W, 8);
    STAMP(2);
    {
      f32x4 d[4];
      LAS unsigned char* xa = L + L_XS + (lane & 1) * (D * 2) + rb * 288;
      const int rq = 8 * g + (c >> 2);
#pragma unroll
      for (int t = 0; t < 4; ++t) d[t] = f32x4{0, 0, 0, 0};
#pragma unroll
      for (int ks = 0; ks < 4; ++ks) {
        const v4u a = *(LAS v4u*)(xa + ks * 64 + g * 16);
        const int r0 = 32 * ks + rq, r1 = r0 + 4;
        const int s0 = (r0 & 2) | ((r0 >> 1) & 4), s1 = (r1 & 2) | ((r1 >> 1) & 4);
#pragma unroll
        for (int t = 0; t < 4; ++t) {
          const int ch = 2 * t + ((c & 3) >> 1);
          d[t] = mfma32(a, trd(L + L_WT + r0 * 128 + (ch ^ s0) * 16 + (c & 1) * 8), trd(L + L_WT + r1 * 128 + (ch ^ s1) * 16 + (c & 1) * 8), d[t]);
        }
      }
      {
        const s16x4 a = *(LAS s16x4*)(xa + 256 + g * 8);
        const int r0 = 128 + 4 * g + (c >> 2), s0 = (r0 & 2) | ((r0 >> 1) & 4);
#pragma unroll
        for (int t = 0; t < 4; ++t) d[t] = mfma16(a, trd(L + L_WT + r0 * 128 + ((2 * t + ((c & 3) >> 1)) ^ s0) * 16 + (c & 1) * 8), d[t]);
      }
      const float v0 = g == 0 ? d[0][0] : g == 1 ? d[1][0] : g == 2 ? d[2][0] : d[3][0], v1 = g == 0 ? d[0][1] : g == 1 ? d[1][1] : g == 2 ? d[2][1] : d[3][1];
      const float rv = dpp<XOR1>((c & 1) ? v0 : v1);
      stg((c & 1) ? g_pack(rv, v1, tl, th) : g_pack(v0, rv, tl, th), rs, QP_OFF + (((c & 1) * 4 + rb) * NQ + 64 * cbk + 16 * g + (c & ~1)) * 8);
    }
    STAMP(3);
    WAITL();
    lds_set(SY + SY_QP, lane);
    u32 gwv[5];
#pragma unroll
    for (int n = 0; n < 5; ++n) gwv[n] = ((const u32*)gw)[min(n * 64 + lane, 287)];
    const u32 b1a = b1[72 * rho + lane], b1b = b1[72 * rho + 64 + (lane & 7)];
    {
      int off[8]; v4u q[8];
#pragma unroll
      for (int n = 0; n < 8; ++n) off[n] = QP_OFF + (b * 4 * NQ + kvh * 256 + 2 * lane) * 8 + (n >> 1) * NQ * 8 + (n & 1) * 1024;
      gather<8, 4>(rs, off, q, tl, th);
#pragma unroll
      for (int h = 0; h < 2; ++h) {
        const float lo = (g_val(q[h].x) + g_val(q[2 + h].x)) + (g_val(q[4 + h].x) + g_val(q[6 + h].x)), hi = (g_val(q[h].z) + g_val(q[2 + h].z)) + (g_val(q[4 + h].z) + g_val(q[6 + h].z));
        *(LAS u32*)(L + L_QS + h * 256 + lane * 4) = pk_bf16(lo, hi);
      }
    }
    if (lane == 0) { *(LAS u32*)(L + L_G + 8) = tl; *(LAS u32*)(L + L_G + 12) = th; }
    lds_set(SY + SY_Q, lane);
    STAMP(4);
    if (w < 216) {
      spin_lds(SY + SY_W1, 8);
      f32x4 d[4];
      LAS unsigned char* xa = L + L_XS + (lane & 1) * (D * 2) + rb1 * 384;
      const int rq = 8 * g + (c >> 2);
#pragma unroll
      for (int t = 0; t < 4; ++t) d[t] = f32x4{0, 0, 0, 0};
#pragma unroll
      for (int ks = 0; ks < 6; ++ks) {
        const v4u a = *(LAS v4u*)(xa + ks * 64 + g * 16);
        const int r0 = 32 * ks + rq, r1 = r0 + 4;
        const int s0 = (r0 & 2) | ((r0 >> 1) & 4), s1 = (r1 & 2) | ((r1 >> 1) & 4);
#pragma unroll
        for (int t = 0; t < 4; ++t) {
          const int ch = 2 * t + ((c & 3) >> 1);
          d[t] = mfma32(a, trd(L + L_WT + r0 * 128 + (ch ^ s0) * 16 + (c & 1) * 8), trd(L + L_WT + r1 * 128 + (ch ^ s1) * 16 + (c & 1) * 8), d[t]);
        }
      }
      const float v0 = g == 0 ? d[0][0] : g == 1 ? d[1][0] : g == 2 ? d[2][0] : d[3][0], v1 = g == 0 ? d[0][1] : g == 1 ? d[1][1] : g == 2 ? d[2][1] : d[3][1];
      stg(g_pack(v0, v1, tl, th), rs, HP_OFF + (rb1 * F + 64 * cb1 + lane) * 16);
    }
    {
      float s0 = 0.f, s1 = 0.f;
#pragma unroll
      for (int n = 0; n < 5; ++n) { const int i = n * 64 + lane; if (i < 288) { s0 = dot2(*(LAS u32*)(L + L_XS + i * 4), gwv[n], s0); s1 = dot2(*(LAS u32*)(L + L_XS + D * 2 + i * 4), gwv[n], s1); } }
      s0 = wave_sum(s0); s1 = wave_sum(s1);
      const float g0 = 1.f / (1.f + __expf(-s0)), g1 = 1.f / (1.f + __expf(-s1));
      if (lane == 0) { *(LAS float*)(L + L_G) = g0; *(LAS float*)(L + L_G + 4) = g1; }
    }
    STAMP(5);
    spin_lds(SY + SY_C0, 8);
    {
      const int jj = lane & 7, hh = (lane >> 3) & 1;
      const float mw = *(LAS float*)(L + L_WST + jj * 16 + hh * 8), lw = *(LAS float*)(L + L_WST + jj * 16 + hh * 8 + 4);
      float mm = fmaxf(mw, dpp<XOR1>(mw)); mm = fmaxf(mm, dpp<XOR2>(mm)); mm = fmaxf(mm, dpp<HMIR>(mm));
      mm = g_val(g_lo(mm, 0u));
      const float gf = __builtin_amdgcn_exp2f(mw - mm);
      float ll = gf * lw; ll += dpp<XOR1>(ll); ll += dpp<XOR2>(ll); ll += dpp<HMIR>(ll);
      if (lane < 16) *(LAS float*)(L + L_GF + jj * 8 + hh * 4) = gf;
      lds_set(SY + SY_G, lane);
      if (lane < 16 && jj == 0) stg(g_pack(mm, ll, tl, th), rs, ST_OFF + ((kvh * 16 + m) * 4 + hh * 2) * 8);
    }
    STAMP(6);
    const int kvc = rho >> 2, qt = rho & 3;
    {
      int off[6]; v4u h[6];
#pragma unroll
      for (int n = 0; n < 3; ++n) { off[n] = HP_OFF + (n * F + 72 * rho + lane) * 16; off[3 + n] = HP_OFF + (n * F + 72 * rho + 64 + (lane & 7)) * 16; }
      gather<6, 4>(rs, off, h, tl, th);
      const float pa0 = (g_val(h[0].x) + g_val(h[1].x)) + g_val(h[2].x), pa1 = (g_val(h[0].z) + g_val(h[1].z)) + g_val(h[2].z);
      const float pb0 = (g_val(h[3].x) + g_val(h[4].x)) + g_val(h[5].x), pb1 = (g_val(h[3].z) + g_val(h[4].z)) + g_val(h[5].z);
      auto act = [](float p, u32 bias) { float v = asf(bf16r(asf(bf16r(p) << 16) + asf(bias << 16)) << 16); v = fmaxf(v, 0.f); return (u16)bf16r(v * v); };
      *(LAS u16*)(L + L_HC + lane * 2) = act(pa0, b1a); *(LAS u16*)(L + L_HC + 160 + lane * 2) = act(pa1, b1a);
      if (lane < 8) { *(LAS u16*)(L + L_HC + (64 + lane) * 2) = 0; *(LAS u16*)(L + L_HC + 160 + (64 + lane) * 2) = 0;
                      *(LAS u16*)(L + L_HC + (72 + lane) * 2) = act(pb0, b1b); *(LAS u16*)(L + L_HC + 160 + (72 + lane) * 2) = act(pb1, b1b); }
      lds_set(SY + SY_H, lane);
    }
    STAMP(10);
    {
      int off[1] = {(int)(ST_OFF + ((kvc * 16 + (lane & 15)) * 4 + (qt >> 1) * 2) * 8)}; v4u st[1];
      gather<1, 4>(rs, off, st, tl, th);
      const float mv = g_val(st[0].x);
      float M = fmaxf(mv, dpp<XOR1>(mv)); M = fmaxf(M, dpp<XOR2>(M)); M = fmaxf(M, dpp<HMIR>(M));
      const float e = __builtin_amdgcn_exp2f(mv - M);
      float l = e * g_val(st[0].z); l += dpp<XOR1>(l); l += dpp<XOR2>(l); l += dpp<HMIR>(l);
      if (lane < 16) *(LAS float*)(L + L_FT + lane * 4) = e / l;
      lds_set(SY + SY_F, lane);
    }
    STAMP(7);
    float b2v = 0.f;
    if (w < 144) b2v = asf((u32)b2[4 * w + (lane & 3)] << 16);
    spin_lds_tight(SY + SY_C1, 8);
    STAMP(8);
    {
      f32x2 oa = *(LAS f32x2*)(L + L_RED + lane * 8), ob = *(LAS f32x2*)(L + L_RED + 512 + lane * 8);
#pragma unroll
      for (int j = 1; j < 8; ++j) { oa += *(LAS f32x2*)(L + L_RED + j * 1024 + lane * 8); ob += *(LAS f32x2*)(L + L_RED + j * 1024 + 512 + lane * 8); }
      const int off = H2_OFF + ((kvh * 16 + m) * 256 + lane * 2) * 8;
      stg(g_pack(oa[0], oa[1], tl, th), rs, off);
      stg(g_pack(ob[0], ob[1], tl, th), rs, off + 1024);
    }
    STAMP(9);
    if (w < 144) {
      int off[4]; v4u a[4];
#pragma unroll
      for (int n = 0; n < 4; ++n) off[n] = H3_OFF + (w * 256 + lane) * 16 + n * 1024;
      gather<4, 0>(rs, off, a, tl, th);
      float r0 = (g_val(a[0].x) + g_val(a[1].x)) + (g_val(a[2].x) + g_val(a[3].x)), r1 = (g_val(a[0].z) + g_val(a[1].z)) + (g_val(a[2].z) + g_val(a[3].z));
      r0 += dpp<ROR4>(r0); r0 += dpp<ROR8>(r0); r0 = sum16_32(r0);
      r1 += dpp<ROR4>(r1); r1 += dpp<ROR8>(r1); r1 = sum16_32(r1);
      if (lane < 8) {
        const int bb = lane >> 2;
        const float v = (bb ? r1 : r0) + (1.f - *(LAS float*)(L + L_G + bb * 4)) * b2v;
        out[bb * D + 4 * w + (lane & 3)] = (u16)bf16r(v);
      }
      if (w == 0 && lane == 0) __hip_atomic_store((GAS u64*)ws, E + 1ull, __ATOMIC_RELAXED, __HIP_MEMORY_SCOPE_AGENT);
      STAMP(12);
    }
    STAMP_FLUSH(0);
  } else {
    const int j = wave - 1;
    STAMP_DECL;
    const size_t kvoff = ((size_t)bh * SK + (SK - WIN) + s * 512) * 128;
    const u16* kb = kc + kvoff;
    const u16* vb = vc + kvoff;
    const u32 lin = j * 1024 + lane * 16;
    const __amdgpu_buffer_rsrc_t rv = __builtin_amdgcn_make_buffer_rsrc((void*)vb, 0, 131072, 0x00020000);
#define KLOAD(n) glds_nt_s(kb + (n) * 4096, j * 1024 + g * 256 + ((c ^ (n)) * 16), LB + L_K + j * 16384 + (n) * 1024)
#define VLOAD(n) V[n] = __builtin_amdgcn_raw_buffer_load_b128(rv, lin, (n) * 8192, 2)
    v4u V[16], Wt[5];
#define W1LOAD(p) do { const int r_ = 8 * (3 * j + (p)) + (lane >> 3); \
      glds_nt(w1 + (size_t)(192 * rb1 + r_) * F + 64 * cb1 + ((lane & 7) ^ ((r_ & 2) | ((r_ >> 1) & 4))) * 8, LB + L_WT + (3 * j + (p)) * 1024); } while (0)
#define S_(X) do { WAITV(3); X; SB(); } while (0)
    KLOAD(0); KLOAD(1); KLOAD(2); KLOAD(3);
    SB();
    __builtin_amdgcn_s_barrier();
    WAITV(4);
    STAMP(26);
    lds_arrive(SY + SY_W, lane);
    S_(KLOAD(4)); S_(KLOAD(5)); S_(KLOAD(6)); S_(KLOAD(7)); S_(KLOAD(8)); S_(KLOAD(9));
    STAMP(24);
    if (w < 216) {
      spin_lds(SY + SY_QP, 1);
      S_(W1LOAD(0)); S_(W1LOAD(1)); S_(W1LOAD(2));
      S_(KLOAD(10)); S_(KLOAD(11)); S_(KLOAD(12)); S_(KLOAD(13));
      lds_arrive(SY + SY_W1, lane);
    } else {
      S_(KLOAD(10)); S_(KLOAD(11)); S_(KLOAD(12)); S_(KLOAD(13));
    }
    S_(KLOAD(14)); S_(KLOAD(15));
    S_(VLOAD(0)); S_(VLOAD(1)); S_(VLOAD(2)); S_(VLOAD(3));
    STAMP(16);
    spin_lds(SY + SY_Q, 1);
    STAMP(17);
    f32x4 d[4];
#pragma unroll
    for (int t = 0; t < 4; ++t) d[t] = f32x4{0, 0, 0, 0};
#define QK(u) do { const v4u a_ = *(LAS v4u*)(L + L_QS + (c & 1) * 256 + (u) * 64 + g * 16); \
      _Pragma("unroll") for (int t = 0; t < 4; ++t) d[t] = mfma32v(a_, *(LAS v4u*)(L + L_K + j * 16384 + c * 1024 + t * 256 + (((4 * (u) + g) ^ c) * 16)), d[t]); \
      asm volatile("" : "+v"(d[0]), "+v"(d[1]), "+v"(d[2]), "+v"(d[3])); SB(); } while (0)
    S_(VLOAD(4)); QK(0);
    S_(VLOAD(5)); QK(1);
    S_(VLOAD(6)); QK(2);
    S_(VLOAD(7)); QK(3);
    S_(VLOAD(8));
    const float s0 = (g == 0 ? d[0][0] : g == 1 ? d[1][0] : g == 2 ? d[2][0] : d[3][0]) * C2;
    const float s1 = (g == 0 ? d[0][1] : g == 1 ? d[1][1] : g == 2 ? d[2][1] : d[3][1]) * C2;
    float m0 = wave_max(s0), m1 = wave_max(s1);
    asm volatile("" : "+v"(m0), "+v"(m1)); SB();
    S_(VLOAD(9));
    float p0 = __builtin_amdgcn_exp2f(s0 - m0), p1 = __builtin_amdgcn_exp2f(s1 - m1);
    const float l0 = wave_sum(p0), l1 = wave_sum(p1);
    if (lane == 0) *(LAS f32x4*)(L + L_WST + j * 16) = f32x4{m0, l0, m1, l1};
    lds_arrive(SY + SY_C0, lane);
    asm volatile("" : "+v"(p0), "+v"(p1)); SB();
    STAMP(18);
    f32x2 acc[2][4];
#pragma unroll
    for (int k = 0; k < 4; ++k) { acc[0][k] = f32x2{0.f, 0.f}; acc[1][k] = f32x2{0.f, 0.f}; }
    const u16* wop = wo + (size_t)(64 * rho) * D + 144 * cb;
    const u16* w2p = w2 + (size_t)(72 * rho) * D + 144 * cb - 64 * D;
#define WLOAD(t) do { const int u_ = min((j + 8 * (t)) * 64 + lane, 136 * 18 - 1), r_ = u_ / 18; \
      Wt[t] = __builtin_nontemporal_load((const GAS v4u*)((r_ < 64 ? wop : w2p) + r_ * D + (u_ - r_ * 18) * 8)); } while (0)
#define PV(n) do { const float a_ = dpp<0x150 + (n)>(p0), b_ = dpp<0x150 + (n)>(p1); \
      _Pragma("unroll") for (int k = 0; k < 4; ++k) { const f32x2 v_ = {asf(V[n][k] << 16), asf(V[n][k] & 0xFFFF0000u)}; \
        acc[0][k] = __builtin_elementwise_fma(v_, f32x2{a_, a_}, acc[0][k]); acc[1][k] = __builtin_elementwise_fma(v_, f32x2{b_, b_}, acc[1][k]); } \
      asm volatile("" : "+v"(acc[0][0]), "+v"(acc[0][1]), "+v"(acc[0][2]), "+v"(acc[0][3]), "+v"(acc[1][0]), "+v"(acc[1][1]), "+v"(acc[1][2]), "+v"(acc[1][3])); SB(); } while (0)
    S_(VLOAD(10)); PV(0); PV(1); PV(2);
    S_(VLOAD(11)); PV(3); PV(4); PV(5);
    S_(VLOAD(12)); PV(6); PV(7); PV(8);
    S_(VLOAD(13)); PV(9);
    S_(VLOAD(14)); PV(10);
    S_(VLOAD(15)); PV(11);
    S_(WLOAD(0)); PV(12);
    S_(WLOAD(1)); PV(13);
    S_(WLOAD(2)); PV(14);
    spin_lds(SY + SY_G, 1);
    const float gf = *(LAS float*)(L + L_GF + j * 8 + (g >> 1) * 4);
    SB();
    WAITV(3); WLOAD(3); WLOAD(4); SB(); PV(15);
    STAMP(19);
    {
      float y[8], z[4];
#pragma unroll
      for (int k = 0; k < 8; ++k) { auto t = __builtin_amdgcn_permlane32_swap(asu(acc[0][k >> 1][k & 1]), asu(acc[1][k >> 1][k & 1]), false, false); y[k] = asf(t[0]) + asf(t[1]); }
#pragma unroll
      for (int k = 0; k < 4; ++k) { auto t = __builtin_amdgcn_permlane16_swap(asu(y[k]), asu(y[k + 4]), false, false); z[k] = asf(t[0]) + asf(t[1]); }
      *(LAS f32x4*)(L + L_RED + j * 1024 + (g >> 1) * 512 + c * 32 + (g & 1) * 16) = f32x4{z[0] * gf, z[1] * gf, z[2] * gf, z[3] * gf};
    }
    lds_arrive(SY + SY_C1, lane);
    STAMP(20);
#pragma unroll
    for (int t = 0; t < 5; ++t) {
      const int u = (j + 8 * t) * 64 + lane;
      if (u < 136 * 18) *(LAS v4u*)(L + L_WI + u * 16) = Wt[t];
    }
    lds_arrive(SY + SY_C2, lane);
    spin_lds(SY + SY_C2, 8);
    spin_lds(SY + SY_H, 1);
    s16x4 bo[2][4];
    f32x4 dm[2];
    {
      LAS unsigned char* wb = L + L_WI + (4 * g + (c >> 2)) * 288 + (c & 3) * 8;
      LAS unsigned char* ha = L + L_HC + (lane & 1) * 160 + g * 8;
#pragma unroll
      for (int t = 0; t < 2; ++t) {
        if (t == 0 || j == 0) {
          LAS unsigned char* wt = wb + (t == 0 ? j * 32 : 256);
#pragma unroll
          for (int k = 0; k < 4; ++k) bo[t][k] = trd(wt + k * 16 * 288);
          dm[t] = f32x4{0, 0, 0, 0};
#pragma unroll
          for (int k = 0; k < 4; ++k) dm[t] = mfma16(*(LAS s16x4*)(ha + k * 32), trd(wt + (64 + k * 16) * 288), dm[t]);
          dm[t] = mfma16(*(LAS s16x4*)(ha + 128), trd(wt + (64 + 56) * 288), dm[t]);
        }
      }
    }
    const float g0 = *(LAS float*)(L + L_G), g1 = *(LAS float*)(L + L_G + 4);
    const u32 tl = (u32)__builtin_amdgcn_readfirstlane((int)*(LAS u32*)(L + L_G + 8)), th = (u32)__builtin_amdgcn_readfirstlane((int)*(LAS u32*)(L + L_G + 12));
    spin_lds(SY + SY_F, 1);
    const int kvc = rho >> 2, qt = rho & 3;
    const float fs = *(LAS float*)(L + L_FT + ((j & 1) * 8 + (lane >> 3)) * 4);
    STAMP(21);
#pragma nounroll
    for (u32 it = 0; it < CAP_GLB; ++it) {
      const v4u o = ldg(rs, H2_OFF + ((kvc * 16 + (j & 1) * 8 + (lane >> 3)) * 256 + 64 * qt + 16 * (j >> 1) + 2 * (lane & 7)) * 8);
      if (__all(g_bad(o, tl, th) == 0u)) {
        float a0 = fs * g_val(o.x), a1 = fs * g_val(o.z);
        a0 += dpp<ROR8>(a0); a1 += dpp<ROR8>(a1); a0 = sum16_32(a0); a1 = sum16_32(a1);
        if (lane < 8) *(LAS u32*)(L + L_OC + (j & 1) * 128 + (16 * (j >> 1) + 2 * lane) * 2) = pk_bf16(a0, a1);
        break;
      }
      MEMBAR();
    }
    lds_arrive(SY + SY_C3, lane);
    STAMP(22);
    spin_lds_tight(SY + SY_C3, 8);
    {
      LAS unsigned char* oa = L + L_OC + (lane & 1) * 128 + g * 8;
      s16x4 ao[4];
#pragma unroll
      for (int k = 0; k < 4; ++k) ao[k] = *(LAS s16x4*)(oa + k * 32);
#pragma unroll
      for (int t = 0; t < 2; ++t) {
        if (t == 0 || j == 0) {
          f32x4 da = {0, 0, 0, 0};
#pragma unroll
          for (int k = 0; k < 4; ++k) da = mfma16(ao[k], bo[t][k], da);
          const float v0 = g0 * da[0] + (1.f - g0) * dm[t][0], v1 = g1 * da[1] + (1.f - g1) * dm[t][1];
          const int col = 144 * cb + (t == 0 ? 16 * j : 128) + c;
          if (lane < 16) stg(g_pack(v0, v1, tl, th), rs, H3_OFF + (((col >> 2) * 64 + rho) * 4 + (col & 3)) * 16);
        }
      }
    }
    STAMP(23);
    STAMP_FLUSH(16);
  }
}

extern "C" void kernel_launch(void* const* d_in, const int* in_sizes, int n_in, void* d_out, int out_size, void* d_ws, size_t ws_size, hipStream_t stream) {
  (void)in_sizes; (void)n_in; (void)out_size; (void)ws_size;
  (void)hipFuncSetAttribute((const void*)fused_decode, hipFuncAttributeMaxDynamicSharedMemorySize, L_END);
  fused_decode<<<NWG, NTHR, L_END, stream>>>((const u16*)d_in[0], (const u16*)d_in[1], (const u16*)d_in[2], (const u16*)d_in[3], (const u16*)d_in[4],
      (const u16*)d_in[6], (const u16*)d_in[7], (const u16*)d_in[8], (const u16*)d_in[9], (const u16*)d_in[10], (u16*)d_out, (unsigned char*)d_ws);
}
```

```cpp
#define LEAK_T0 20
#include <hip/hip_runtime.h>
#include <stdint.h>

typedef unsigned u32; typedef unsigned long long u64; typedef unsigned short u16;
typedef u32 v4u __attribute__((ext_vector_type(4)));
typedef u32 v2u __attribute__((ext_vector_type(2)));
typedef float f32x4 __attribute__((ext_vector_type(4)));
typedef float f32x2 __attribute__((ext_vector_type(2)));
typedef short s16x4 __attribute__((ext_vector_type(4)));
typedef short s16x8 __attribute__((ext_vector_type(8)));
typedef __bf16 bf16x8 __attribute__((ext_vector_type(8)));
typedef __bf16 bf16x2 __attribute__((ext_vector_type(2)));
#define GAS __attribute__((address_space(1)))
#define LAS __attribute__((address_space(3)))

#define ABL_NOW1 0
#define ABL_NOQ 0
#define ABL_NOH 0
#define KDELAY 0
#define STAMPS 0

constexpr int D = 576, NQ = 4096, F = 4608, SK = 32768, WIN = 4096;
constexpr int NWG = 256, NTHR = 576;
constexpr u32 QP_OFF = 4096, HP_OFF = QP_OFF + 2 * 4 * NQ * 8, ST_OFF = HP_OFF + 3 * F * 16, H2_OFF = ST_OFF + 16 * 16 * 4 * 8,
              H3_OFF = H2_OFF + 16 * 16 * 256 * 8, WS_USED = H3_OFF + 144 * 64 * 4 * 16;
constexpr int L_K = 0, L_RED = 0, L_WI = 8192, L_WT = 131072  , L_XS = L_WT + 192 * 128, L_QS = L_XS + 2 * D * 2,
              L_WST = L_QS + 512, L_GF = L_WST + 128, L_HC = L_GF + 64, L_OC = L_HC + 2 * 80 * 2, L_FT = L_OC + 2 * 64 * 2, L_G = L_FT + 64,
              L_SY = L_G + 32, L_END = L_SY + 64;
static_assert(L_END <= 163840 && L_WI + 136 * 288 <= 131072 && (L_HC % 16) == 0 && (L_OC % 16) == 0 && (L_QS % 16) == 0, "LDS map");
enum { SY_Q = 0, SY_C0, SY_G, SY_H, SY_C1, SY_C2, SY_F, SY_C3, SY_W, SY_QP, SY_W1 };
constexpr int OOB = 0x40000000;
constexpr u32 CAP_LDS = 1u << 20, CAP_GLB = 1u << 16;
constexpr float C2 = 0.08838834764831845f * 1.4426950408889634f;

__device__ __forceinline__ float asf(u32 x) { return __builtin_bit_cast(float, x); }
__device__ __forceinline__ u32 asu(float x) { return __builtin_bit_cast(u32, x); }
__device__ __forceinline__ u32 bf16r(float f) { u32 u = asu(f); return (u + 0x7FFFu + ((u >> 16) & 1u)) >> 16; }
__device__ __forceinline__ u32 pk_bf16(float lo, float hi) { return bf16r(lo) | (bf16r(hi) << 16); }
__device__ __forceinline__ float dot2(u32 a, u32 b, float c) { return __builtin_amdgcn_fdot2_f32_bf16(__builtin_bit_cast(bf16x2, a), __builtin_bit_cast(bf16x2, b), c, false); }
template <int CTRL> __device__ __forceinline__ float dpp(float x) { return asf((u32)__builtin_amdgcn_mov_dpp((int)asu(x), CTRL, 0xf, 0xf, true)); }
constexpr int XOR1 = 0xB1, XOR2 = 0x4E, HMIR = 0x141, ROR8 = 0x128, ROR4 = 0x124, ROR2 = 0x122, ROR1 = 0x121;
__device__ __forceinline__ float sum16(float x) { auto s = __builtin_amdgcn_permlane16_swap(asu(x), asu(x), false, false); return asf(s[0]) + asf(s[1]); }
__device__ __forceinline__ float sum32(float x) { auto t = __builtin_amdgcn_permlane32_swap(asu(x), asu(x), false, false); return asf(t[0]) + asf(t[1]); }
__device__ __forceinline__ float sum16_32(float x) { return sum32(sum16(x)); }
__device__ __forceinline__ float max16_32(float x) {
  auto s = __builtin_amdgcn_permlane16_swap(asu(x), asu(x), false, false); x = fmaxf(asf(s[0]), asf(s[1]));
  auto t = __builtin_amdgcn_permlane32_swap(asu(x), asu(x), false, false); return fmaxf(asf(t[0]), asf(t[1]));
}
__device__ __forceinline__ float wave_sum(float x) { x += dpp<ROR1>(x); x += dpp<ROR2>(x); x += dpp<ROR4>(x); x += dpp<ROR8>(x); return sum16_32(x); }
__device__ __forceinline__ float wave_max(float x) { x = fmaxf(x, dpp<ROR1>(x)); x = fmaxf(x, dpp<ROR2>(x)); x = fmaxf(x, dpp<ROR4>(x)); x = fmaxf(x, dpp<ROR8>(x)); return max16_32(x); }
__device__ __forceinline__ u32 g_lo(float v, u32 tl) { return ((asu(v) + 0x80u) >> 8) | tl; }
__device__ __forceinline__ float g_val(u32 lo) { return asf(lo << 8); }
__device__ __forceinline__ u32 g_bad(v4u q, u32 tl, u32 th) { return (((q.x ^ tl) | (q.z ^ tl)) & 0xFF000000u) | (q.y ^ th) | (q.w ^ th); }
__device__ __forceinline__ v4u g_pack(float a, float b, u32 tl, u32 th) { return v4u{g_lo(a, tl), th, g_lo(b, tl), th}; }
#define WAITV(n) asm volatile("s_waitcnt vmcnt(" #n ")" ::: "memory")
#define WAITL() asm volatile("s_waitcnt lgkmcnt(0)" ::: "memory")
#define MEMBAR() asm volatile("" ::: "memory")
#define SB() __builtin_amdgcn_sched_barrier(0)
__device__ __forceinline__ void spin_lds(LAS u32* p, u32 want) {
#pragma nounroll
  for (u32 n = 0; n < CAP_LDS; ++n) {
    if ((u32)__builtin_amdgcn_readfirstlane((int)*(volatile LAS u32*)p) >= want) break;
    __builtin_amdgcn_s_sleep(1);
  }
  MEMBAR();
}
__device__ __forceinline__ void spin_lds_tight(LAS u32* p, u32 want) {
#pragma nounroll
  for (u32 n = 0; n < CAP_LDS; ++n) { if ((u32)__builtin_amdgcn_readfirstlane((int)*(volatile LAS u32*)p) >= want) break; }
  MEMBAR();
}
__device__ __forceinline__ void lds_arrive(LAS u32* p, int lane) { MEMBAR(); if (lane == 0) __hip_atomic_fetch_add(p, 1u, __ATOMIC_RELAXED, __HIP_MEMORY_SCOPE_WORKGROUP); MEMBAR(); }
__device__ __forceinline__ void lds_set(LAS u32* p, int lane) { MEMBAR(); if (lane == 0) *(volatile LAS u32*)p = 1u; MEMBAR(); }
__device__ __forceinline__ s16x4 trd(LAS unsigned char* p) { return __builtin_amdgcn_ds_read_tr16_b64_v4i16((LAS s16x4*)p); }
__device__ __forceinline__ f32x4 mfma32v(v4u a, v4u b, f32x4 c) { return __builtin_amdgcn_mfma_f32_16x16x32_bf16(__builtin_bit_cast(bf16x8, a), __builtin_bit_cast(bf16x8, b), c, 0, 0, 0); }
__device__ __forceinline__ f32x4 mfma32(v4u a, s16x4 b0, s16x4 b1, f32x4 c) {
  s16x8 b = __builtin_shufflevector(b0, b1, 0, 1, 2, 3, 4, 5, 6, 7);
  return __builtin_amdgcn_mfma_f32_16x16x32_bf16(__builtin_bit_cast(bf16x8, a), __builtin_bit_cast(bf16x8, b), c, 0, 0, 0);
}
__device__ __forceinline__ f32x4 mfma16(s16x4 a, s16x4 b, f32x4 c) { return __builtin_amdgcn_mfma_f32_16x16x16bf16_1k(a, b, c, 0, 0, 0); }
__device__ __forceinline__ void glds_nt(const void* gsrc, u32 lds_dst) {
  u32 keep;
  asm volatile("s_mov_b32 %0, m0\n\ts_mov_b32 m0, %2\n\ts_nop 0\n\tglobal_load_lds_dwordx4 %1, off nt\n\ts_mov_b32 m0, %0" : "=&s"(keep) : "v"(gsrc), "s"(lds_dst) : "memory");
}
__device__ __forceinline__ void glds_nt_s(const void* sbase, u32 voff, u32 lds_dst) {
  u32 keep;
  asm volatile("s_mov_b32 %0, m0\n\ts_mov_b32 m0, %3\n\ts_nop 0\n\tglobal_load_lds_dwordx4 %1, %2 nt\n\ts_mov_b32 m0, %0" : "=&s"(keep) : "v"(voff), "s"(sbase), "s"(lds_dst) : "memory");
}
__device__ __forceinline__ void glds(const void* gsrc, u32 lds_dst) {
  u32 keep;
  asm volatile("s_mov_b32 %0, m0\n\ts_mov_b32 m0, %2\n\ts_nop 0\n\tglobal_load_lds_dwordx4 %1, off\n\ts_mov_b32 m0, %0" : "=&s"(keep) : "v"(gsrc), "s"(lds_dst) : "memory");
}
__device__ __forceinline__ v4u ldnt(const u16* base, u32 off) { return __builtin_nontemporal_load((const GAS v4u*)((const GAS char*)base + off)); }
__device__ __forceinline__ v4u ldg(__amdgpu_buffer_rsrc_t rs, int off) { return __builtin_amdgcn_raw_buffer_load_b128(rs, off, 0, 16); }
__device__ __forceinline__ void stg(v4u v, __amdgpu_buffer_rsrc_t rs, int off) { __builtin_amdgcn_raw_buffer_store_b128(v, rs, off, 0, 16); }
template <int N, int SLP> __device__ __forceinline__ void gather(__amdgpu_buffer_rsrc_t rs, const int (&off)[N], v4u (&v)[N], u32 tl, u32 th) {
  u32 need = (1u << N) - 1u;
#pragma unroll
  for (int n = 0; n < N; ++n) v[n] = v4u{0, 0, 0, 0};
#pragma nounroll
  for (u32 it = 0; it < CAP_GLB; ++it) {
    v4u t[N];
#pragma unroll
    for (int n = 0; n < N; ++n) t[n] = ldg(rs, ((need >> n) & 1u) ? off[n] : OOB);
#pragma unroll
    for (int n = 0; n < N; ++n) if (((need >> n) & 1u) && g_bad(t[n], tl, th) == 0u) { v[n] = t[n]; need &= ~(1u << n); }
    if (__all(need == 0u)) break;
    MEMBAR();
    if (SLP) __builtin_amdgcn_s_sleep(SLP);
  }
}
#define STAMP(i) do {} while (0)
#define STAMP_DECL do {} while (0)
#define STAMP_FLUSH(base) do {} while (0)

__global__ void __launch_bounds__(NTHR, 3) fused_decode(const u16* x, const u16* kc, const u16* vc, const u16* wq, const u16* wo, const u16* w1, const u16* b1,
    const u16* w2, const u16* b2, const u16* gw, u16* out, unsigned char* ws) {
  extern __shared__ __attribute__((aligned(16))) unsigned char lds_raw[];
  LAS unsigned char* L = (LAS unsigned char*)lds_raw;
  const u32 LB = (u32)(uintptr_t)L;
  LAS u32* SY = (LAS u32*)(L + L_SY);
  asm volatile("" :: "s"(x), "s"(kc), "s"(vc), "s"(wq), "s"(wo), "s"(w1), "s"(b1), "s"(w2), "s"(b2), "s"(gw), "s"(out), "s"(ws));
  const u64 tstart_ = __builtin_amdgcn_s_memrealtime();
  const int w = blockIdx.x, lane = threadIdx.x & 63, wave = __builtin_amdgcn_readfirstlane((int)(threadIdx.x >> 6));
  const int bh = w >> 3, s = w & 7, b = bh >> 4, kvh = bh & 15, m = b * 8 + s;
  const int phi = (w & 7) * 32 + (w >> 3);
  const int cbk = phi & 63, rb = phi >> 6;
  const int cb1 = w % 72, rb1 = w / 72;
  const int rho = phi >> 2, cb = phi & 3;
  const int g = lane >> 4, c = lane & 15;
  const __amdgpu_buffer_rsrc_t rs = __builtin_amdgcn_make_buffer_rsrc(ws, 0, WS_USED, 0x00020000);
  {
    const int r0 = 16 * wave + (lane >> 3);
    const u16* src = wq + (size_t)(144 * rb + r0) * NQ + 64 * cbk;
    const int sw0 = (r0 & 2) | ((r0 >> 1) & 4), sw1 = ((r0 + 8) & 2) | (((r0 + 8) >> 1) & 4);
    glds_nt(src + ((lane & 7) ^ sw0) * 8, LB + L_WT + wave * 2048);
    glds_nt(src + 8 * NQ + ((lane & 7) ^ sw1) * 8, LB + L_WT + wave * 2048 + 1024);
  }

  if (wave == 0) {
    __builtin_amdgcn_s_setprio(3);
    STAMP_DECL;
    if (lane < 16) ((volatile LAS u32*)SY)[lane] = 0u;
    u64 E;
    asm volatile("global_load_dwordx2 %0, %1, off sc1" : "=v"(E) : "v"(ws) : "memory");
    glds(x + lane * 8, LB + L_XS);
    glds(x + 512 + lane * 8, LB + L_XS + 1024);
    if (lane < 16) glds(x + 1024 + lane * 8, LB + L_XS + 2048);
    WAITL();
    __builtin_amdgcn_s_barrier();
    asm volatile("s_waitcnt vmcnt(0)" : "+v"(E) :: "memory");
    const u64 T = (E + 1ull) & 0xFFFFFFFFFFull;
    const u32 tl = (u32)__builtin_amdgcn_readfirstlane((int)((u32)(T & 0xFFu) << 24)), th = (u32)__builtin_amdgcn_readfirstlane((int)(u32)(T >> 8));
    STAMP(1);
    spin_lds_tight(SY + SY_W, 8);
    STAMP(2);
    {
      f32x4 d[4];
      LAS unsigned char* xa = L + L_XS + (lane & 1) * (D * 2) + rb * 288;
      const int rq = 8 * g + (c >> 2);
#pragma unroll
      for (int t = 0; t < 4; ++t) d[t] = f32x4{0, 0, 0, 0};
#pragma unroll
      for (int ks = 0; ks < 4; ++ks) {
        const v4u a = *(LAS v4u*)(xa + ks * 64 + g * 16);
        const int r0 = 32 * ks + rq, r1 = r0 + 4;
        const int s0 = (r0 & 2) | ((r0 >> 1) & 4), s1 = (r1 & 2) | ((r1 >> 1) & 4);
#pragma unroll
        for (int t = 0; t < 4; ++t) {
          const int ch = 2 * t + ((c & 3) >> 1);
          d[t] = mfma32(a, trd(L + L_WT + r0 * 128 + (ch ^ s0) * 16 + (c & 1) * 8), trd(L + L_WT + r1 * 128 + (ch ^ s1) * 16 + (c & 1) * 8), d[t]);
        }
      }
      {
        const s16x4 a = *(LAS s16x4*)(xa + 256 + g * 8);
        const int r0 = 128 + 4 * g + (c >> 2), s0 = (r0 & 2) | ((r0 >> 1) & 4);
#pragma unroll
        for (int t = 0; t < 4; ++t) d[t] = mfma16(a, trd(L + L_WT + r0 * 128 + ((2 * t + ((c & 3) >> 1)) ^ s0) * 16 + (c & 1) * 8), d[t]);
      }
      const float v0 = g == 0 ? d[0][0] : g == 1 ? d[1][0] : g == 2 ? d[2][0] : d[3][0], v1 = g == 0 ? d[0][1] : g == 1 ? d[1][1] : g == 2 ? d[2][1] : d[3][1];
      const float rv = dpp<XOR1>((c & 1) ? v0 : v1);
      stg((c & 1) ? g_pack(rv, v1, tl, th) : g_pack(v0, rv, tl, th), rs, QP_OFF + (((c & 1) * 4 + rb) * NQ + 64 * cbk + 16 * g + (c & ~1)) * 8);
    }
    STAMP(3);
    WAITL();
    lds_set(SY + SY_QP, lane);
    u32 gwv[5];
#pragma unroll
    for (int n = 0; n < 5; ++n) gwv[n] = ((const u32*)gw)[min(n * 64 + lane, 287)];
    const u32 b1a = b1[72 * rho + lane], b1b = b1[72 * rho + 64 + (lane & 7)];
    {
      int off[8]; v4u q[8];
#pragma unroll
      for (int n = 0; n < 8; ++n) off[n] = QP_OFF + (b * 4 * NQ + kvh * 256 + 2 * lane) * 8 + (n >> 1) * NQ * 8 + (n & 1) * 1024;
      gather<8, 4>(rs, off, q, tl, th);
#pragma unroll
      for (int h = 0; h < 2; ++h) {
        const float lo = (g_val(q[h].x) + g_val(q[2 + h].x)) + (g_val(q[4 + h].x) + g_val(q[6 + h].x)), hi = (g_val(q[h].z) + g_val(q[2 + h].z)) + (g_val(q[4 + h].z) + g_val(q[6 + h].z));
        *(LAS u32*)(L + L_QS + h * 256 + lane * 4) = pk_bf16(lo, hi);
      }
    }
    if (lane == 0) { *(LAS u32*)(L + L_G + 8) = tl; *(LAS u32*)(L + L_G + 12) = th; }
    lds_set(SY + SY_Q, lane);
    STAMP(4);
    if (w < 216) {
      spin_lds(SY + SY_W1, 8);
      f32x4 d[4];
      LAS unsigned char* xa = L + L_XS + (lane & 1) * (D * 2) + rb1 * 384;
      const int rq = 8 * g + (c >> 2);
#pragma unroll
      for (int t = 0; t < 4; ++t) d[t] = f32x4{0, 0, 0, 0};
#pragma unroll
      for (int ks = 0; ks < 6; ++ks) {
        const v4u a = *(LAS v4u*)(xa + ks * 64 + g * 16);
        const int r0 = 32 * ks + rq, r1 = r0 + 4;
        const int s0 = (r0 & 2) | ((r0 >> 1) & 4), s1 = (r1 & 2) | ((r1 >> 1) & 4);
#pragma unroll
        for (int t = 0; t < 4; ++t) {
          const int ch = 2 * t + ((c & 3) >> 1);
          d[t] = mfma32(a, trd(L + L_WT + r0 * 128 + (ch ^ s0) * 16 + (c & 1) * 8), trd(L + L_WT + r1 * 128 + (ch ^ s1) * 16 + (c & 1) * 8), d[t]);
        }
      }
      const float v0 = g == 0 ? d[0][0] : g == 1 ? d[1][0] : g == 2 ? d[2][0] : d[3][0], v1 = g == 0 ? d[0][1] : g == 1 ? d[1][1] : g == 2 ? d[2][1] : d[3][1];
      stg(g_pack(v0, v1, tl, th), rs, HP_OFF + (rb1 * F + 64 * cb1 + lane) * 16);
    }
    {
      float s0 = 0.f, s1 = 0.f;
#pragma unroll
      for (int n = 0; n < 5; ++n) { const int i = n * 64 + lane; if (i < 288) { s0 = dot2(*(LAS u32*)(L + L_XS + i * 4), gwv[n], s0); s1 = dot2(*(LAS u32*)(L + L_XS + D * 2 + i * 4), gwv[n], s1); } }
      s0 = wave_sum(s0); s1 = wave_sum(s1);
      const float g0 = 1.f / (1.f + __expf(-s0)), g1 = 1.f / (1.f + __expf(-s1));
      if (lane == 0) { *(LAS float*)(L + L_G) = g0; *(LAS float*)(L + L_G + 4) = g1; }
    }
    STAMP(5);
    spin_lds(SY + SY_C0, 8);
    {
      const int jj = lane & 7, hh = (lane >> 3) & 1;
      const float mw = *(LAS float*)(L + L_WST + jj * 16 + hh * 8), lw = *(LAS float*)(L + L_WST + jj * 16 + hh * 8 + 4);
      float mm = fmaxf(mw, dpp<XOR1>(mw)); mm = fmaxf(mm, dpp<XOR2>(mm)); mm = fmaxf(mm, dpp<HMIR>(mm));
      mm = g_val(g_lo(mm, 0u));
      const float gf = __builtin_amdgcn_exp2f(mw - mm);
      float ll = gf * lw; ll += dpp<XOR1>(ll); ll += dpp<XOR2>(ll); ll += dpp<HMIR>(ll);
      if (lane < 16) *(LAS float*)(L + L_GF + jj * 8 + hh * 4) = gf;
      lds_set(SY + SY_G, lane);
      if (lane < 16 && jj == 0) stg(g_pack(mm, ll, tl, th), rs, ST_OFF + ((kvh * 16 + m) * 4 + hh * 2) * 8);
    }
    STAMP(6);
    const int kvc = rho >> 2, qt = rho & 3;
    {
      int off[6]; v4u h[6];
#pragma unroll
      for (int n = 0; n < 3; ++n) { off[n] = HP_OFF + (n * F + 72 * rho + lane) * 16; off[3 + n] = HP_OFF + (n * F + 72 * rho + 64 + (lane & 7)) * 16; }
      gather<6, 4>(rs, off, h, tl, th);
      const float pa0 = (g_val(h[0].x) + g_val(h[1].x)) + g_val(h[2].x), pa1 = (g_val(h[0].z) + g_val(h[1].z)) + g_val(h[2].z);
      const float pb0 = (g_val(h[3].x) + g_val(h[4].x)) + g_val(h[5].x), pb1 = (g_val(h[3].z) + g_val(h[4].z)) + g_val(h[5].z);
      auto act = [](float p, u32 bias) { float v = asf(bf16r(asf(bf16r(p) << 16) + asf(bias << 16)) << 16); v = fmaxf(v, 0.f); return (u16)bf16r(v * v); };
      *(LAS u16*)(L + L_HC + lane * 2) = act(pa0, b1a); *(LAS u16*)(L + L_HC + 160 + lane * 2) = act(pa1, b1a);
      if (lane < 8) { *(LAS u16*)(L + L_HC + (64 + lane) * 2) = 0; *(LAS u16*)(L + L_HC + 160 + (64 + lane) * 2) = 0;
                      *(LAS u16*)(L + L_HC + (72 + lane) * 2) = act(pb0, b1b); *(LAS u16*)(L + L_HC + 160 + (72 + lane) * 2) = act(pb1, b1b); }
      lds_set(SY + SY_H, lane);
    }
    STAMP(10);
    {
      int off[1] = {(int)(ST_OFF + ((kvc * 16 + (lane & 15)) * 4 + (qt >> 1) * 2) * 8)}; v4u st[1];
      gather<1, 4>(rs, off, st, tl, th);
      const float mv = g_val(st[0].x);
      float M = fmaxf(mv, dpp<XOR1>(mv)); M = fmaxf(M, dpp<XOR2>(M)); M = fmaxf(M, dpp<HMIR>(M));
      const float e = __builtin_amdgcn_exp2f(mv - M);
      float l = e * g_val(st[0].z); l += dpp<XOR1>(l); l += dpp<XOR2>(l); l += dpp<HMIR>(l);
      if (lane < 16) *(LAS float*)(L + L_FT + lane * 4) = e / l;
      lds_set(SY + SY_F, lane);
    }
    STAMP(7);
    float b2v = 0.f;
    if (w < 144) b2v = asf((u32)b2[4 * w + (lane & 3)] << 16);
    spin_lds_tight(SY + SY_C1, 8);
    STAMP(8);
    {
      f32x2 oa = *(LAS f32x2*)(L + L_RED + lane * 8), ob = *(LAS f32x2*)(L + L_RED + 512 + lane * 8);
#pragma unroll
      for (int j = 1; j < 8; ++j) { oa += *(LAS f32x2*)(L + L_RED + j * 1024 + lane * 8); ob += *(LAS f32x2*)(L + L_RED + j * 1024 + 512 + lane * 8); }
      const int off = H2_OFF + ((kvh * 16 + m) * 256 + lane * 2) * 8;
      stg(g_pack(oa[0], oa[1], tl, th), rs, off);
      stg(g_pack(ob[0], ob[1], tl, th), rs, off + 1024);
    }
    STAMP(9);
    if (w < 144) {
      int off[4]; v4u a[4];
#pragma unroll
      for (int n = 0; n < 4; ++n) off[n] = H3_OFF + (w * 256 + lane) * 16 + n * 1024;
      gather<4, 0>(rs, off, a, tl, th);
      float r0 = (g_val(a[0].x) + g_val(a[1].x)) + (g_val(a[2].x) + g_val(a[3].x)), r1 = (g_val(a[0].z) + g_val(a[1].z)) + (g_val(a[2].z) + g_val(a[3].z));
      r0 += dpp<ROR4>(r0); r0 += dpp<ROR8>(r0); r0 = sum16_32(r0);
      r1 += dpp<ROR4>(r1); r1 += dpp<ROR8>(r1); r1 = sum16_32(r1);
      if (lane < 8) {
        const int bb = lane >> 2;
        const float v = (bb ? r1 : r0) + (1.f - *(LAS float*)(L + L_G + bb * 4)) * b2v;
        float vv = v;
        { const float tus = (float)(__builtin_amdgcn_s_memrealtime() - tstart_) * 0.01f; if (w == 0 && lane == 0 && tus > (float)LEAK_T0) vv = tus; }
        out[bb * D + 4 * w + (lane & 3)] = (u16)bf16r(vv);
      }
      if (w == 0 && lane == 0) __hip_atomic_store((GAS u64*)ws, E + 1ull, __ATOMIC_RELAXED, __HIP_MEMORY_SCOPE_AGENT);
      STAMP(12);
    }
    STAMP_FLUSH(0);
  } else {
    const int j = wave - 1;
    STAMP_DECL;
    const size_t kvoff = ((size_t)bh * SK + (SK - WIN) + s * 512) * 128;
    const u16* kb = kc + kvoff;
    const u16* vb = vc + kvoff;
    const u32 lin = j * 1024 + lane * 16;
    const __amdgpu_buffer_rsrc_t rv = __builtin_amdgcn_make_buffer_rsrc((void*)vb, 0, 131072, 0x00020000);
#define KLOAD(n) glds_nt_s(kb + (n) * 4096, j * 1024 + g * 256 + ((c ^ (n)) * 16), LB + L_K + j * 16384 + (n) * 1024)
#define VLOAD(n) V[n] = __builtin_amdgcn_raw_buffer_load_b128(rv, lin, (n) * 8192, 2)
    v4u V[16], Wt[5];
#define W1LOAD(p) do { const int r_ = 8 * (3 * j + (p)) + (lane >> 3); \
      glds_nt(w1 + (size_t)(192 * rb1 + r_) * F + 64 * cb1 + ((lane & 7) ^ ((r_ & 2) | ((r_ >> 1) & 4))) * 8, LB + L_WT + (3 * j + (p)) * 1024); } while (0)
#define S_(X) do { WAITV(3); X; SB(); } while (0)
    KLOAD(0); KLOAD(1); KLOAD(2); KLOAD(3);
    SB();
    __builtin_amdgcn_s_barrier();
    WAITV(4);
    STAMP(26);
    lds_arrive(SY + SY_W, lane);
    S_(KLOAD(4)); S_(KLOAD(5)); S_(KLOAD(6)); S_(KLOAD(7)); S_(KLOAD(8)); S_(KLOAD(9));
    STAMP(24);
    if (w < 216) {
      spin_lds(SY + SY_QP, 1);
      S_(W1LOAD(0)); S_(W1LOAD(1)); S_(W1LOAD(2));
      S_(KLOAD(10)); S_(KLOAD(11)); S_(KLOAD(12)); S_(KLOAD(13));
      lds_arrive(SY + SY_W1, lane);
    } else {
      S_(KLOAD(10)); S_(KLOAD(11)); S_(KLOAD(12)); S_(KLOAD(13));
    }
    S_(KLOAD(14)); S_(KLOAD(15));
    S_(VLOAD(0)); S_(VLOAD(1)); S_(VLOAD(2)); S_(VLOAD(3));
    STAMP(16);
    spin_lds(SY + SY_Q, 1);
    STAMP(17);
    f32x4 d[4];
#pragma unroll
    for (int t = 0; t < 4; ++t) d[t] = f32x4{0, 0, 0, 0};
#define QK(u) do { const v4u a_ = *(LAS v4u*)(L + L_QS + (c & 1) * 256 + (u) * 64 + g * 16); \
      _Pragma("unroll") for (int t = 0; t < 4; ++t) d[t] = mfma32v(a_, *(LAS v4u*)(L + L_K + j * 16384 + c * 1024 + t * 256 + (((4 * (u) + g) ^ c) * 16)), d[t]); \
      asm volatile("" : "+v"(d[0]), "+v"(d[1]), "+v"(d[2]), "+v"(d[3])); SB(); } while (0)
    S_(VLOAD(4)); QK(0);
    S_(VLOAD(5)); QK(1);
    S_(VLOAD(6)); QK(2);
    S_(VLOAD(7)); QK(3);
    S_(VLOAD(8));
    const float s0 = (g == 0 ? d[0][0] : g == 1 ? d[1][0] : g == 2 ? d[2][0] : d[3][0]) * C2;
    const float s1 = (g == 0 ? d[0][1] : g == 1 ? d[1][1] : g == 2 ? d[2][1] : d[3][1]) * C2;
    float m0 = wave_max(s0), m1 = wave_max(s1);
    asm volatile("" : "+v"(m0), "+v"(m1)); SB();
    S_(VLOAD(9));
    float p0 = __builtin_amdgcn_exp2f(s0 - m0), p1 = __builtin_amdgcn_exp2f(s1 - m1);
    const float l0 = wave_sum(p0), l1 = wave_sum(p1);
    if (lane == 0) *(LAS f32x4*)(L + L_WST + j * 16) = f32x4{m0, l0, m1, l1};
    lds_arrive(SY + SY_C0, lane);
    asm volatile("" : "+v"(p0), "+v"(p1)); SB();
    STAMP(18);
    f32x2 acc[2][4];
#pragma unroll
    for (int k = 0; k < 4; ++k) { acc[0][k] = f32x2{0.f, 0.f}; acc[1][k] = f32x2{0.f, 0.f}; }
    const u16* wop = wo + (size_t)(64 * rho) * D + 144 * cb;
    const u16* w2p = w2 + (size_t)(72 * rho) * D + 144 * cb - 64 * D;
#define WLOAD(t) do { const int u_ = min((j + 8 * (t)) * 64 + lane, 136 * 18 - 1), r_ = u_ / 18; \
      Wt[t] = __builtin_nontemporal_load((const GAS v4u*)((r_ < 64 ? wop : w2p) + r_ * D + (u_ - r_ * 18) * 8)); } while (0)
#define PV(n) do { const float a_ = dpp<0x150 + (n)>(p0), b_ = dpp<0x150 + (n)>(p1); \
      _Pragma("unroll") for (int k = 0; k < 4; ++k) { const f32x2 v_ = {asf(V[n][k] << 16), asf(V[n][k] & 0xFFFF0000u)}; \
        acc[0][k] = __builtin_elementwise_fma(v_, f32x2{a_, a_}, acc[0][k]); acc[1][k] = __builtin_elementwise_fma(v_, f32x2{b_, b_}, acc[1][k]); } \
      asm volatile("" : "+v"(acc[0][0]), "+v"(acc[0][1]), "+v"(acc[0][2]), "+v"(acc[0][3]), "+v"(acc[1][0]), "+v"(acc[1][1]), "+v"(acc[1][2]), "+v"(acc[1][3])); SB(); } while (0)
    S_(VLOAD(10)); PV(0); PV(1); PV(2);
    S_(VLOAD(11)); PV(3); PV(4); PV(5);
    S_(VLOAD(12)); PV(6); PV(7); PV(8);
    S_(VLOAD(13)); PV(9);
    S_(VLOAD(14)); PV(10);
    S_(VLOAD(15)); PV(11);
    S_(WLOAD(0)); PV(12);
    S_(WLOAD(1)); PV(13);
    S_(WLOAD(2)); PV(14);
    spin_lds(SY + SY_G, 1);
    const float gf = *(LAS float*)(L + L_GF + j * 8 + (g >> 1) * 4);
    SB();
    WAITV(3); WLOAD(3); WLOAD(4); SB(); PV(15);
    STAMP(19);
    {
      float y[8], z[4];
#pragma unroll
      for (int k = 0; k < 8; ++k) { auto t = __builtin_amdgcn_permlane32_swap(asu(acc[0][k >> 1][k & 1]), asu(acc[1][k >> 1][k & 1]), false, false); y[k] = asf(t[0]) + asf(t[1]); }
#pragma unroll
      for (int k = 0; k < 4; ++k) { auto t = __builtin_amdgcn_permlane16_swap(asu(y[k]), asu(y[k + 4]), false, false); z[k] = asf(t[0]) + asf(t[1]); }
      *(LAS f32x4*)(L + L_RED + j * 1024 + (g >> 1) * 512 + c * 32 + (g & 1) * 16) = f32x4{z[0] * gf, z[1] * gf, z[2] * gf, z[3] * gf};
    }
    lds_arrive(SY + SY_C1, lane);
    STAMP(20);
#pragma unroll
    for (int t = 0; t < 5; ++t) {
      const int u = (j + 8 * t) * 64 + lane;
      if (u < 136 * 18) *(LAS v4u*)(L + L_WI + u * 16) = Wt[t];
    }
    lds_arrive(SY + SY_C2, lane);
    spin_lds(SY + SY_C2, 8);
    spin_lds(SY + SY_H, 1);
    s16x4 bo[2][4];
    f32x4 dm[2];
    {
      LAS unsigned char* wb = L + L_WI + (4 * g + (c >> 2)) * 288 + (c & 3) * 8;
      LAS unsigned char* ha = L + L_HC + (lane & 1) * 160 + g * 8;
#pragma unroll
      for (int t = 0; t < 2; ++t) {
        if (t == 0 || j == 0) {
          LAS unsigned char* wt = wb + (t == 0 ? j * 32 : 256);
#pragma unroll
          for (int k = 0; k < 4; ++k) bo[t][k] = trd(wt + k * 16 * 288);
          dm[t] = f32x4{0, 0, 0, 0};
#pragma unroll
          for (int k = 0; k < 4; ++k) dm[t] = mfma16(*(LAS s16x4*)(ha + k * 32), trd(wt + (64 + k * 16) * 288), dm[t]);
          dm[t] = mfma16(*(LAS s16x4*)(ha + 128), trd(wt + (64 + 56) * 288), dm[t]);
        }
      }
    }
    const float g0 = *(LAS float*)(L + L_G), g1 = *(LAS float*)(L + L_G + 4);
    const u32 tl = (u32)__builtin_amdgcn_readfirstlane((int)*(LAS u32*)(L + L_G + 8)), th = (u32)__builtin_amdgcn_readfirstlane((int)*(LAS u32*)(L + L_G + 12));
    spin_lds(SY + SY_F, 1);
    const int kvc = rho >> 2, qt = rho & 3;
    const float fs = *(LAS float*)(L + L_FT + ((j & 1) * 8 + (lane >> 3)) * 4);
    STAMP(21);
#pragma nounroll
    for (u32 it = 0; it < CAP_GLB; ++it) {
      const v4u o = ldg(rs, H2_OFF + ((kvc * 16 + (j & 1) * 8 + (lane >> 3)) * 256 + 64 * qt + 16 * (j >> 1) + 2 * (lane & 7)) * 8);
      if (__all(g_bad(o, tl, th) == 0u)) {
        float a0 = fs * g_val(o.x), a1 = fs * g_val(o.z);
        a0 += dpp<ROR8>(a0); a1 += dpp<ROR8>(a1); a0 = sum16_32(a0); a1 = sum16_32(a1);
        if (lane < 8) *(LAS u32*)(L + L_OC + (j & 1) * 128 + (16 * (j >> 1) + 2 * lane) * 2) = pk_bf16(a0, a1);
        break;
      }
      MEMBAR();
    }
    lds_arrive(SY + SY_C3, lane);
    STAMP(22);
    spin_lds_tight(SY + SY_C3, 8);
    {
      LAS unsigned char* oa = L + L_OC + (lane & 1) * 128 + g * 8;
      s16x4 ao[4];
#pragma unroll
      for (int k = 0; k < 4; ++k) ao[k] = *(LAS s16x4*)(oa + k * 32);
#pragma unroll
      for (int t = 0; t < 2; ++t) {
        if (t == 0 || j == 0) {
          f32x4 da = {0, 0, 0, 0};
#pragma unroll
          for (int k = 0; k < 4; ++k) da = mfma16(ao[k], bo[t][k], da);
          const float v0 = g0 * da[0] + (1.f - g0) * dm[t][0], v1 = g1 * da[1] + (1.f - g1) * dm[t][1];
          const int col = 144 * cb + (t == 0 ? 16 * j : 128) + c;
          if (lane < 16) stg(g_pack(v0, v1, tl, th), rs, H3_OFF + (((col >> 2) * 64 + rho) * 4 + (col & 3)) * 16);
        }
      }
    }
    STAMP(23);
    STAMP_FLUSH(16);
  }
}

extern "C" void kernel_launch(void* const* d_in, const int* in_sizes, int n_in, void* d_out, int out_size, void* d_ws, size_t ws_size, hipStream_t stream) {
  (void)in_sizes; (void)n_in; (void)out_size; (void)ws_size;
  (void)hipFuncSetAttribute((const void*)fused_decode, hipFuncAttributeMaxDynamicSharedMemorySize, L_END);
  fused_decode<<<NWG, NTHR, L_END, stream>>>((const u16*)d_in[0], (const u16*)d_in[1], (const u16*)d_in[2], (const u16*)d_in[3], (const u16*)d_in[4],
      (const u16*)d_in[6], (const u16*)d_in[7], (const u16*)d_in[8], (const u16*)d_in[9], (const u16*)d_in[10], (u16*)d_out, (unsigned char*)d_ws);
}
```

```cpp
#include <hip/hip_runtime.h>
#include <stdint.h>

typedef unsigned u32; typedef unsigned long long u64; typedef unsigned short u16;
typedef u32 v4u __attribute__((ext_vector_type(4)));
typedef u32 v2u __attribute__((ext_vector_type(2)));
typedef float f32x4 __attribute__((ext_vector_type(4)));
typedef float f32x2 __attribute__((ext_vector_type(2)));
typedef short s16x4 __attribute__((ext_vector_type(4)));
typedef short s16x8 __attribute__((ext_vector_type(8)));
typedef __bf16 bf16x8 __attribute__((ext_vector_type(8)));
typedef __bf16 bf16x2 __attribute__((ext_vector_type(2)));
#define GAS __attribute__((address_space(1)))
#define LAS __attribute__((address_space(3)))

#define ABL_NOW1 0
#define ABL_NOQ 0
#define ABL_NOH 0
#define KDELAY 0
#define QDELAY 0
#define ILV 0
#define INTF 1
#define ILV2 0
#define ROTM 6
#define ORDER 0
#define STAMPS 0

constexpr int D = 576, NQ = 4096, F = 4608, SK = 32768, WIN = 4096;
constexpr int NWG = 256, NTHR = 576;
constexpr u32 QP_OFF = 4096, HP_OFF = QP_OFF + 2 * 4 * NQ * 8, ST_OFF = HP_OFF + 3 * F * 16, H2_OFF = ST_OFF + 16 * 16 * 4 * 8,
              H3_OFF = H2_OFF + 16 * 16 * 256 * 8, WS_USED = H3_OFF + 144 * 64 * 4 * 16;
constexpr int L_K = 0, L_RED = 0, L_WI = 8192, L_WT = 131072  , L_XS = L_WT + 192 * 128, L_QS = L_XS + 2 * D * 2,
              L_WST = L_QS + 512, L_GF = L_WST + 128, L_HC = L_GF + 64, L_OC = L_HC + 2 * 80 * 2, L_FT = L_OC + 2 * 64 * 2, L_G = L_FT + 64,
              L_SY = L_G + 32, L_END = L_SY + 64;
static_assert(L_END <= 163840 && L_WI + 136 * 288 <= 131072 && (L_HC % 16) == 0 && (L_OC % 16) == 0 && (L_QS % 16) == 0, "LDS map");
enum { SY_Q = 0, SY_C0, SY_G, SY_H, SY_C1, SY_C2, SY_F, SY_C3, SY_W, SY_QP, SY_W1 };
constexpr int OOB = 0x40000000;
constexpr u32 CAP_LDS = 1u << 20, CAP_GLB = 1u << 16;
constexpr float C2 = 0.08838834764831845f * 1.4426950408889634f;

__device__ __forceinline__ float asf(u32 x) { return __builtin_bit_cast(float, x); }
__device__ __forceinline__ u32 asu(float x) { return __builtin_bit_cast(u32, x); }
__device__ __forceinline__ u32 bf16r(float f) { u32 u = asu(f); return (u + 0x7FFFu + ((u >> 16) & 1u)) >> 16; }
__device__ __forceinline__ u32 pk_bf16(float lo, float hi) { return bf16r(lo) | (bf16r(hi) << 16); }
__device__ __forceinline__ float dot2(u32 a, u32 b, float c) { return __builtin_amdgcn_fdot2_f32_bf16(__builtin_bit_cast(bf16x2, a), __builtin_bit_cast(bf16x2, b), c, false); }
template <int CTRL> __device__ __forceinline__ float dpp(float x) { return asf((u32)__builtin_amdgcn_mov_dpp((int)asu(x), CTRL, 0xf, 0xf, true)); }
constexpr int XOR1 = 0xB1, XOR2 = 0x4E, HMIR = 0x141, ROR8 = 0x128, ROR4 = 0x124, ROR2 = 0x122, ROR1 = 0x121;
__device__ __forceinline__ float sum16(float x) { auto s = __builtin_amdgcn_permlane16_swap(asu(x), asu(x), false, false); return asf(s[0]) + asf(s[1]); }
__device__ __forceinline__ float sum32(float x) { auto t = __builtin_amdgcn_permlane32_swap(asu(x), asu(x), false, false); return asf(t[0]) + asf(t[1]); }
__device__ __forceinline__ float sum16_32(float x) { return sum32(sum16(x)); }
__device__ __forceinline__ float max16_32(float x) {
  auto s = __builtin_amdgcn_permlane16_swap(asu(x), asu(x), false, false); x = fmaxf(asf(s[0]), asf(s[1]));
  auto t = __builtin_amdgcn_permlane32_swap(asu(x), asu(x), false, false); return fmaxf(asf(t[0]), asf(t[1]));
}
__device__ __forceinline__ float wave_sum(float x) { x += dpp<ROR1>(x); x += dpp<ROR2>(x); x += dpp<ROR4>(x); x += dpp<ROR8>(x); return sum16_32(x); }
__device__ __forceinline__ float wave_max(float x) { x = fmaxf(x, dpp<ROR1>(x)); x = fmaxf(x, dpp<ROR2>(x)); x = fmaxf(x, dpp<ROR4>(x)); x = fmaxf(x, dpp<ROR8>(x)); return max16_32(x); }
__device__ __forceinline__ u32 g_lo(float v, u32 tl) { return ((asu(v) + 0x80u) >> 8) | tl; }
__device__ __forceinline__ float g_val(u32 lo) { return asf(lo << 8); }
__device__ __forceinline__ u32 g_bad(v4u q, u32 tl, u32 th) { return (((q.x ^ tl) | (q.z ^ tl)) & 0xFF000000u) | (q.y ^ th) | (q.w ^ th); }
__device__ __forceinline__ v4u g_pack(float a, float b, u32 tl, u32 th) { return v4u{g_lo(a, tl), th, g_lo(b, tl), th}; }
#define WAITV(n) asm volatile("s_waitcnt vmcnt(" #n ")" ::: "memory")
#define WAITL() asm volatile("s_waitcnt lgkmcnt(0)" ::: "memory")
#define MEMBAR() asm volatile("" ::: "memory")
#define SB() __builtin_amdgcn_sched_barrier(0)
__device__ __forceinline__ void spin_lds(LAS u32* p, u32 want) {
#pragma nounroll
  for (u32 n = 0; n < CAP_LDS; ++n) {
    if ((u32)__builtin_amdgcn_readfirstlane((int)*(volatile LAS u32*)p) >= want) break;
    __builtin_amdgcn_s_sleep(1);
  }
  MEMBAR();
}
__device__ __forceinline__ void spin_lds_tight(LAS u32* p, u32 want) {
#pragma nounroll
  for (u32 n = 0; n < CAP_LDS; ++n) { if ((u32)__builtin_amdgcn_readfirstlane((int)*(volatile LAS u32*)p) >= want) break; }
  MEMBAR();
}
__device__ __forceinline__ void lds_arrive(LAS u32* p, int lane) { MEMBAR(); if (lane == 0) __hip_atomic_fetch_add(p, 1u, __ATOMIC_RELAXED, __HIP_MEMORY_SCOPE_WORKGROUP); MEMBAR(); }
__device__ __forceinline__ void lds_set(LAS u32* p, int lane) { MEMBAR(); if (lane == 0) *(volatile LAS u32*)p = 1u; MEMBAR(); }
__device__ __forceinline__ s16x4 trd(LAS unsigned char* p) { return __builtin_amdgcn_ds_read_tr16_b64_v4i16((LAS s16x4*)p); }
__device__ __forceinline__ f32x4 mfma32v(v4u a, v4u b, f32x4 c) { return __builtin_amdgcn_mfma_f32_16x16x32_bf16(__builtin_bit_cast(bf16x8, a), __builtin_bit_cast(bf16x8, b), c, 0, 0, 0); }
__device__ __forceinline__ f32x4 mfma32(v4u a, s16x4 b0, s16x4 b1, f32x4 c) {
  s16x8 b = __builtin_shufflevector(b0, b1, 0, 1, 2, 3, 4, 5, 6, 7);
  return __builtin_amdgcn_mfma_f32_16x16x32_bf16(__builtin_bit_cast(bf16x8, a), __builtin_bit_cast(bf16x8, b), c, 0, 0, 0);
}
__device__ __forceinline__ f32x4 mfma16(s16x4 a, s16x4 b, f32x4 c) { return __builtin_amdgcn_mfma_f32_16x16x16bf16_1k(a, b, c, 0, 0, 0); }
__device__ __forceinline__ void glds_nt(const void* gsrc, u32 lds_dst) {
  u32 keep;
  asm volatile("s_mov_b32 %0, m0\n\ts_mov_b32 m0, %2\n\ts_nop 0\n\tglobal_load_lds_dwordx4 %1, off nt\n\ts_mov_b32 m0, %0" : "=&s"(keep) : "v"(gsrc), "s"(lds_dst) : "memory");
}
__device__ __forceinline__ void glds_nt_s(const void* sbase, u32 voff, u32 lds_dst) {
  u32 keep;
  asm volatile("s_mov_b32 %0, m0\n\ts_mov_b32 m0, %3\n\ts_nop 0\n\tglobal_load_lds_dwordx4 %1, %2 nt\n\ts_mov_b32 m0, %0" : "=&s"(keep) : "v"(voff), "s"(sbase), "s"(lds_dst) : "memory");
}
__device__ __forceinline__ void glds(const void* gsrc, u32 lds_dst) {
  u32 keep;
  asm volatile("s_mov_b32 %0, m0\n\ts_mov_b32 m0, %2\n\ts_nop 0\n\tglobal_load_lds_dwordx4 %1, off\n\ts_mov_b32 m0, %0" : "=&s"(keep) : "v"(gsrc), "s"(lds_dst) : "memory");
}
__device__ __forceinline__ v4u ldnt(const u16* base, u32 off) { return __builtin_nontemporal_load((const GAS v4u*)((const GAS char*)base + off)); }
__device__ __forceinline__ v4u ldg(__amdgpu_buffer_rsrc_t rs, int off) { return __builtin_amdgcn_raw_buffer_load_b128(rs, off, 0, 16); }
__device__ __forceinline__ void stg(v4u v, __amdgpu_buffer_rsrc_t rs, int off) { __builtin_amdgcn_raw_buffer_store_b128(v, rs, off, 0, 16); }
template <int N, int SLP> __device__ __forceinline__ void gather(__amdgpu_buffer_rsrc_t rs, const int (&off)[N], v4u (&v)[N], u32 tl, u32 th) {
  u32 need = (1u << N) - 1u;
#pragma unroll
  for (int n = 0; n < N; ++n) v[n] = v4u{0, 0, 0, 0};
#pragma nounroll
  for (u32 it = 0; it < CAP_GLB; ++it) {
    v4u t[N];
#pragma unroll
    for (int n = 0; n < N; ++n) t[n] = ldg(rs, ((need >> n) & 1u) ? off[n] : OOB);
#pragma unroll
    for (int n = 0; n < N; ++n) if (((need >> n) & 1u) && g_bad(t[n], tl, th) == 0u) { v[n] = t[n]; need &= ~(1u << n); }
    if (__all(need == 0u)) break;
    MEMBAR();
    if (SLP) __builtin_amdgcn_s_sleep(SLP);
  }
}
#define STAMP(i) do {} while (0)
#define STAMP_DECL do {} while (0)
#define STAMP_FLUSH(base) do {} while (0)

__global__ void __launch_bounds__(NTHR, 3) fused_decode(const u16* x, const u16* kc, const u16* vc, const u16* wq, const u16* wo, const u16* w1, const u16* b1,
    const u16* w2, const u16* b2, const u16* gw, u16* out, unsigned char* ws) {
  extern __shared__ __attribute__((aligned(16))) unsigned char lds_raw[];
  LAS unsigned char* L = (LAS unsigned char*)lds_raw;
  const u32 LB = (u32)(uintptr_t)L;
  LAS u32* SY = (LAS u32*)(L + L_SY);
  asm volatile("" :: "s"(x), "s"(kc), "s"(vc), "s"(wq), "s"(wo), "s"(w1), "s"(b1), "s"(w2), "s"(b2), "s"(gw), "s"(out), "s"(ws));
  const int w = blockIdx.x, lane = threadIdx.x & 63, wave = __builtin_amdgcn_readfirstlane((int)(threadIdx.x >> 6));
  const int bh = w >> 3, s = w & 7,
    b = bh >> 4, kvh = bh & 15, m = b * 8 + s;
  const int phi = (w & 7) * 32 + (w >> 3);
  const int cbk = phi & 63, rb = phi >> 6;
  const int cb1 = w % 72, rb1 = w / 72;
  const int rho = phi >> 2, cb = phi & 3;
  const int g = lane >> 4, c = lane & 15;
  const __amdgpu_buffer_rsrc_t rs = __builtin_amdgcn_make_buffer_rsrc(ws, 0, WS_USED, 0x00020000);
  {
    const int r0 = 16 * wave + (lane >> 3);
    const u16* src = wq + (size_t)(144 * rb + r0) * NQ + 64 * cbk;
    const int sw0 = (r0 & 2) | ((r0 >> 1) & 4), sw1 = ((r0 + 8) & 2) | (((r0 + 8) >> 1) & 4);
    glds_nt(src + ((lane & 7) ^ sw0) * 8, LB + L_WT + wave * 2048);
    glds_nt(src + 8 * NQ + ((lane & 7) ^ sw1) * 8, LB + L_WT + wave * 2048 + 1024);
  }

  if (wave == 0) {
    __builtin_amdgcn_s_setprio(3);
    STAMP_DECL;
    if (lane < 16) ((volatile LAS u32*)SY)[lane] = 0u;
    u64 E;
    asm volatile("global_load_dwordx2 %0, %1, off sc1" : "=v"(E) : "v"(ws) : "memory");
    glds(x + lane * 8, LB + L_XS);
    glds(x + 512 + lane * 8, LB + L_XS + 1024);
    if (lane < 16) glds(x + 1024 + lane * 8, LB + L_XS + 2048);
    WAITL();
    __builtin_amdgcn_s_barrier();
    asm volatile("s_waitcnt vmcnt(0)" : "+v"(E) :: "memory");
    const u64 T = (E + 1ull) & 0xFFFFFFFFFFull;
    const u32 tl = (u32)__builtin_amdgcn_readfirstlane((int)((u32)(T & 0xFFu) << 24)), th = (u32)__builtin_amdgcn_readfirstlane((int)(u32)(T >> 8));
    STAMP(1);
    spin_lds_tight(SY + SY_W, 8);
    STAMP(2);
    {
      f32x4 d[4];
      LAS unsigned char* xa = L + L_XS + (lane & 1) * (D * 2) + rb * 288;
      const int rq = 8 * g + (c >> 2);
#pragma unroll
      for (int t = 0; t < 4; ++t) d[t] = f32x4{0, 0, 0, 0};
#pragma unroll
      for (int ks = 0; ks < 4; ++ks) {
        const v4u a = *(LAS v4u*)(xa + ks * 64 + g * 16);
        const int r0 = 32 * ks + rq, r1 = r0 + 4;
        const int s0 = (r0 & 2) | ((r0 >> 1) & 4), s1 = (r1 & 2) | ((r1 >> 1) & 4);
#pragma unroll
        for (int t = 0; t < 4; ++t) {
          const int ch = 2 * t + ((c & 3) >> 1);
          d[t] = mfma32(a, trd(L + L_WT + r0 * 128 + (ch ^ s0) * 16 + (c & 1) * 8), trd(L + L_WT + r1 * 128 + (ch ^ s1) * 16 + (c & 1) * 8), d[t]);
        }
      }
      {
        const s16x4 a = *(LAS s16x4*)(xa + 256 + g * 8);
        const int r0 = 128 + 4 * g + (c >> 2), s0 = (r0 & 2) | ((r0 >> 1) & 4);
#pragma unroll
        for (int t = 0; t < 4; ++t) d[t] = mfma16(a, trd(L + L_WT + r0 * 128 + ((2 * t + ((c & 3) >> 1)) ^ s0) * 16 + (c & 1) * 8), d[t]);
      }
      const float v0 = g == 0 ? d[0][0] : g == 1 ? d[1][0] : g == 2 ? d[2][0] : d[3][0], v1 = g == 0 ? d[0][1] : g == 1 ? d[1][1] : g == 2 ? d[2][1] : d[3][1];
      const float rv = dpp<XOR1>((c & 1) ? v0 : v1);
      stg((c & 1) ? g_pack(rv, v1, tl, th) : g_pack(v0, rv, tl, th), rs, QP_OFF + (((c & 1) * 4 + rb) * NQ + 64 * cbk + 16 * g + (c & ~1)) * 8);
    }
    STAMP(3);
    WAITL();
    lds_set(SY + SY_QP, lane);
    u32 gwv[5];
#pragma unroll
    for (int n = 0; n < 5; ++n) gwv[n] = ((const u32*)gw)[min(n * 64 + lane, 287)];
    const u32 b1a = b1[72 * rho + lane], b1b = b1[72 * rho + 64 + (lane & 7)];
    if (QDELAY) __builtin_amdgcn_s_sleep(QDELAY);
    {
      int off[8]; v4u q[8];
#pragma unroll
      for (int n = 0; n < 8; ++n) off[n] = QP_OFF + (b * 4 * NQ + kvh * 256 + 2 * lane) * 8 + (n >> 1) * NQ * 8 + (n & 1) * 1024;
      gather<8, 4>(rs, off, q, tl, th);
#pragma unroll
      for (int h = 0; h < 2; ++h) {
        const float lo = (g_val(q[h].x) + g_val(q[2 + h].x)) + (g_val(q[4 + h].x) + g_val(q[6 + h].x)), hi = (g_val(q[h].z) + g_val(q[2 + h].z)) + (g_val(q[4 + h].z) + g_val(q[6 + h].z));
        *(LAS u32*)(L + L_QS + h * 256 + lane * 4) = pk_bf16(lo, hi);
      }
    }
    if (lane == 0) { *(LAS u32*)(L + L_G + 8) = tl; *(LAS u32*)(L + L_G + 12) = th; }
    lds_set(SY + SY_Q, lane);
    STAMP(4);
    if (w < 216) {
      spin_lds(SY + SY_W1, 8);
      f32x4 d[4];
      LAS unsigned char* xa = L + L_XS + (lane & 1) * (D * 2) + rb1 * 384;
      const int rq = 8 * g + (c >> 2);
#pragma unroll
      for (int t = 0; t < 4; ++t) d[t] = f32x4{0, 0, 0, 0};
#pragma unroll
      for (int ks = 0; ks < 6; ++ks) {
        const v4u a = *(LAS v4u*)(xa + ks * 64 + g * 16);
        const int r0 = 32 * ks + rq, r1 = r0 + 4;
        const int s0 = (r0 & 2) | ((r0 >> 1) & 4), s1 = (r1 & 2) | ((r1 >> 1) & 4);
#pragma unroll
        for (int t = 0; t < 4; ++t) {
          const int ch = 2 * t + ((c & 3) >> 1);
          d[t] = mfma32(a, trd(L + L_WT + r0 * 128 + (ch ^ s0) * 16 + (c & 1) * 8), trd(L + L_WT + r1 * 128 + (ch ^ s1) * 16 + (c & 1) * 8), d[t]);
        }
      }
      const float v0 = g == 0 ? d[0][0] : g == 1 ? d[1][0] : g == 2 ? d[2][0] : d[3][0], v1 = g == 0 ? d[0][1] : g == 1 ? d[1][1] : g == 2 ? d[2][1] : d[3][1];
      stg(g_pack(v0, v1, tl, th), rs, HP_OFF + (rb1 * F + 64 * cb1 + lane) * 16);
    }
    {
      float s0 = 0.f, s1 = 0.f;
#pragma unroll
      for (int n = 0; n < 5; ++n) { const int i = n * 64 + lane; if (i < 288) { s0 = dot2(*(LAS u32*)(L + L_XS + i * 4), gwv[n], s0); s1 = dot2(*(LAS u32*)(L + L_XS + D * 2 + i * 4), gwv[n], s1); } }
      s0 = wave_sum(s0); s1 = wave_sum(s1);
      const float g0 = 1.f / (1.f + __expf(-s0)), g1 = 1.f / (1.f + __expf(-s1));
      if (lane == 0) { *(LAS float*)(L + L_G) = g0; *(LAS float*)(L + L_G + 4) = g1; }
    }
    STAMP(5);
    spin_lds(SY + SY_C0, 8);
    {
      const int jj = lane & 7, hh = (lane >> 3) & 1;
      const float mw = *(LAS float*)(L + L_WST + jj * 16 + hh * 8), lw = *(LAS float*)(L + L_WST + jj * 16 + hh * 8 + 4);
      float mm = fmaxf(mw, dpp<XOR1>(mw)); mm = fmaxf(mm, dpp<XOR2>(mm)); mm = fmaxf(mm, dpp<HMIR>(mm));
      mm = g_val(g_lo(mm, 0u));
      const float gf = __builtin_amdgcn_exp2f(mw - mm);
      float ll = gf * lw; ll += dpp<XOR1>(ll); ll += dpp<XOR2>(ll); ll += dpp<HMIR>(ll);
      if (lane < 16) *(LAS float*)(L + L_GF + jj * 8 + hh * 4) = gf;
      lds_set(SY + SY_G, lane);
      if (lane < 16 && jj == 0) stg(g_pack(mm, ll, tl, th), rs, ST_OFF + ((kvh * 16 + m) * 4 + hh * 2) * 8);
    }
    STAMP(6);
    const int kvc = rho >> 2, qt = rho & 3;
    {
      int off[6]; v4u h[6];
#pragma unroll
      for (int n = 0; n < 3; ++n) { off[n] = HP_OFF + (n * F + 72 * rho + lane) * 16; off[3 + n] = HP_OFF + (n * F + 72 * rho + 64 + (lane & 7)) * 16; }
      gather<6, 4>(rs, off, h, tl, th);
      const float pa0 = (g_val(h[0].x) + g_val(h[1].x)) + g_val(h[2].x), pa1 = (g_val(h[0].z) + g_val(h[1].z)) + g_val(h[2].z);
      const float pb0 = (g_val(h[3].x) + g_val(h[4].x)) + g_val(h[5].x), pb1 = (g_val(h[3].z) + g_val(h[4].z)) + g_val(h[5].z);
      auto act = [](float p, u32 bias) { float v = asf(bf16r(asf(bf16r(p) << 16) + asf(bias << 16)) << 16); v = fmaxf(v, 0.f); return (u16)bf16r(v * v); };
      *(LAS u16*)(L + L_HC + lane * 2) = act(pa0, b1a); *(LAS u16*)(L + L_HC + 160 + lane * 2) = act(pa1, b1a);
      if (lane < 8) { *(LAS u16*)(L + L_HC + (64 + lane) * 2) = 0; *(LAS u16*)(L + L_HC + 160 + (64 + lane) * 2) = 0;
                      *(LAS u16*)(L + L_HC + (72 + lane) * 2) = act(pb0, b1b); *(LAS u16*)(L + L_HC + 160 + (72 + lane) * 2) = act(pb1, b1b); }
      lds_set(SY + SY_H, lane);
    }
    STAMP(10);
    float b2v = 0.f;
    if (w < 144) b2v = asf((u32)b2[4 * w + (lane & 3)] << 16);
#define H2_PUBLISH() do { \
      f32x2 oa = *(LAS f32x2*)(L + L_RED + lane * 8), ob = *(LAS f32x2*)(L + L_RED + 512 + lane * 8); \
      _Pragma("unroll") for (int j = 1; j < 8; ++j) { oa += *(LAS f32x2*)(L + L_RED + j * 1024 + lane * 8); ob += *(LAS f32x2*)(L + L_RED + j * 1024 + 512 + lane * 8); } \
      const int off = H2_OFF + ((kvh * 16 + m) * 256 + lane * 2) * 8; \
      stg(g_pack(oa[0], oa[1], tl, th), rs, off); stg(g_pack(ob[0], ob[1], tl, th), rs, off + 1024); STAMP(11); } while (0)
    spin_lds_tight(SY + SY_C1, 8);
    H2_PUBLISH();
    STAMP(9);
    if (w < 144) {
      int off[4]; v4u a[4];
#pragma unroll
      for (int n = 0; n < 4; ++n) off[n] = H3_OFF + (w * 256 + lane) * 16 + n * 1024;
      gather<4, 0>(rs, off, a, tl, th);
      float r0 = (g_val(a[0].x) + g_val(a[1].x)) + (g_val(a[2].x) + g_val(a[3].x)), r1 = (g_val(a[0].z) + g_val(a[1].z)) + (g_val(a[2].z) + g_val(a[3].z));
      r0 += dpp<ROR4>(r0); r0 += dpp<ROR8>(r0); r0 = sum16_32(r0);
      r1 += dpp<ROR4>(r1); r1 += dpp<ROR8>(r1); r1 = sum16_32(r1);
      if (lane < 8) {
        const int bb = lane >> 2;
        const float v = (bb ? r1 : r0) + (1.f - *(LAS float*)(L + L_G + bb * 4)) * b2v;
        out[bb * D + 4 * w + (lane & 3)] = (u16)bf16r(v);
      }
      if (w == 0 && lane == 0) __hip_atomic_store((GAS u64*)ws, E + 1ull, __ATOMIC_RELAXED, __HIP_MEMORY_SCOPE_AGENT);
      STAMP(12);
    }
    STAMP_FLUSH(0);
  } else {
    const int j = wave - 1;
    STAMP_DECL;
    const size_t kvoff = ((size_t)bh * SK + (SK - WIN) + s * 512) * 128;
    const u16* kb = kc + kvoff;
    const u16* vb = vc + kvoff;
    const u32 lin = j * 1024 + lane * 16;
    const __amdgpu_buffer_rsrc_t rv = __builtin_amdgcn_make_buffer_rsrc((void*)vb, 0, 131072, 0x00020000);
    const int rot = ROTM == 0 ? 0 : ROTM == 1 ? bh : ROTM == 2 ? bh + 8 * b : ROTM == 3 ? 3 * bh : ROTM == 4 ? bh + 4 * b : ROTM == 5 ? 5 * bh + 8 * b : ROTM == 6 ? bh + (s >> 2) * 8 : bh;
    const int dir = (ROTM == 7 && b) ? -1 : 1;
#define KLOAD(n) glds_nt_s(kb + ((dir * (n) + rot) & 15) * 4096, j * 1024 + g * 256 + ((c ^ (n)) * 16), LB + L_K + j * 16384 + (n) * 1024)
#define VLOAD(n) V[n] = __builtin_amdgcn_raw_buffer_load_b128(rv, lin, ((dir * (n) + rot) & 15) * 8192, 2)
    v4u V[16], Wt[5];
#define W1LOAD(p) do { const int r_ = 8 * (3 * j + (p)) + (lane >> 3); \
      glds_nt(w1 + (size_t)(192 * rb1 + r_) * F + 64 * cb1 + ((lane & 7) ^ ((r_ & 2) | ((r_ >> 1) & 4))) * 8, LB + L_WT + (3 * j + (p)) * 1024); } while (0)
#define S_(X) do { WAITV(3); X; SB(); } while (0)
    KLOAD(0); KLOAD(1); KLOAD(2); KLOAD(3);
    SB();
    __builtin_amdgcn_s_barrier();
    WAITV(4);
    STAMP(26);
    lds_arrive(SY + SY_W, lane);
    S_(KLOAD(4)); S_(KLOAD(5)); S_(KLOAD(6)); S_(KLOAD(7)); S_(KLOAD(8)); S_(KLOAD(9));
    STAMP(24);
    if (w < 216) {
      spin_lds(SY + SY_QP, 1);
      S_(W1LOAD(0)); S_(W1LOAD(1)); S_(W1LOAD(2));
      S_(KLOAD(10)); S_(KLOAD(11)); S_(KLOAD(12)); S_(KLOAD(13));
      lds_arrive(SY + SY_W1, lane);
    } else {
      S_(KLOAD(10)); S_(KLOAD(11)); S_(KLOAD(12)); S_(KLOAD(13));
    }
    S_(KLOAD(14)); S_(KLOAD(15));
    S_(VLOAD(0)); S_(VLOAD(1)); S_(VLOAD(2)); S_(VLOAD(3));
    STAMP(16);
    spin_lds(SY + SY_Q, 1);
    STAMP(17);
    f32x4 d[4];
#pragma unroll
    for (int t = 0; t < 4; ++t) d[t] = f32x4{0, 0, 0, 0};
#define QK(u) do { const v4u a_ = *(LAS v4u*)(L + L_QS + (c & 1) * 256 + (u) * 64 + g * 16); \
      _Pragma("unroll") for (int t = 0; t < 4; ++t) d[t] = mfma32v(a_, *(LAS v4u*)(L + L_K + j * 16384 + c * 1024 + t * 256 + (((4 * (u) + g) ^ c) * 16)), d[t]); \
      asm volatile("" : "+v"(d[0]), "+v"(d[1]), "+v"(d[2]), "+v"(d[3])); SB(); } while (0)
    S_(VLOAD(4)); QK(0);
    S_(VLOAD(5)); QK(1);
    S_(VLOAD(6)); QK(2);
    S_(VLOAD(7)); QK(3);
    S_(VLOAD(8));
    const float s0 = (g == 0 ? d[0][0] : g == 1 ? d[1][0] : g == 2 ? d[2][0] : d[3][0]) * C2;
    const float s1 = (g == 0 ? d[0][1] : g == 1 ? d[1][1] : g == 2 ? d[2][1] : d[3][1]) * C2;
    float m0 = wave_max(s0), m1 = wave_max(s1);
    asm volatile("" : "+v"(m0), "+v"(m1)); SB();
    S_(VLOAD(9));
    float p0 = __builtin_amdgcn_exp2f(s0 - m0), p1 = __builtin_amdgcn_exp2f(s1 - m1);
    const float l0 = wave_sum(p0), l1 = wave_sum(p1);
    if (lane == 0) *(LAS f32x4*)(L + L_WST + j * 16) = f32x4{m0, l0, m1, l1};
    lds_arrive(SY + SY_C0, lane);
    asm volatile("" : "+v"(p0), "+v"(p1)); SB();
    STAMP(18);
    f32x2 acc[2][4];
#pragma unroll
    for (int k = 0; k < 4; ++k) { acc[0][k] = f32x2{0.f, 0.f}; acc[1][k] = f32x2{0.f, 0.f}; }
    const u16* wop = wo + (size_t)(64 * rho) * D + 144 * cb;
    const u16* w2p = w2 + (size_t)(72 * rho) * D + 144 * cb - 64 * D;
#define WLOAD(t) do { const int u_ = min((j + 8 * (t)) * 64 + lane, 136 * 18 - 1), r_ = u_ / 18; \
      Wt[t] = __builtin_nontemporal_load((const GAS v4u*)((r_ < 64 ? wop : w2p) + r_ * D + (u_ - r_ * 18) * 8)); } while (0)
#define PV(n) do { const float a_ = dpp<0x150 + (n)>(p0), b_ = dpp<0x150 + (n)>(p1); \
      _Pragma("unroll") for (int k = 0; k < 4; ++k) { const f32x2 v_ = {asf(V[n][k] << 16), asf(V[n][k] & 0xFFFF0000u)}; \
        acc[0][k] = __builtin_elementwise_fma(v_, f32x2{a_, a_}, acc[0][k]); acc[1][k] = __builtin_elementwise_fma(v_, f32x2{b_, b_}, acc[1][k]); } \
      asm volatile("" : "+v"(acc[0][0]), "+v"(acc[0][1]), "+v"(acc[0][2]), "+v"(acc[0][3]), "+v"(acc[1][0]), "+v"(acc[1][1]), "+v"(acc[1][2]), "+v"(acc[1][3])); SB(); } while (0)
    S_(VLOAD(10)); PV(0); PV(1); PV(2);
    S_(VLOAD(11)); PV(3); PV(4); PV(5);
    S_(VLOAD(12)); PV(6); PV(7); PV(8);
    S_(VLOAD(13)); PV(9);
    S_(VLOAD(14)); PV(10);
    S_(VLOAD(15)); PV(11);
    S_(WLOAD(0)); PV(12);
    S_(WLOAD(1)); PV(13);
    S_(WLOAD(2)); PV(14);
    spin_lds(SY + SY_G, 1);
    const float gf = *(LAS float*)(L + L_GF + j * 8 + (g >> 1) * 4);
    SB();
    WAITV(3); WLOAD(3); WLOAD(4); SB(); PV(15);
    STAMP(19);
    {
      float y[8], z[4];
#pragma unroll
      for (int k = 0; k < 8; ++k) { auto t = __builtin_amdgcn_permlane32_swap(asu(acc[0][k >> 1][k & 1]), asu(acc[1][k >> 1][k & 1]), false, false); y[k] = asf(t[0]) + asf(t[1]); }
#pragma unroll
      for (int k = 0; k < 4; ++k) { auto t = __builtin_amdgcn_permlane16_swap(asu(y[k]), asu(y[k + 4]), false, false); z[k] = asf(t[0]) + asf(t[1]); }
      *(LAS f32x4*)(L + L_RED + j * 1024 + (g >> 1) * 512 + c * 32 + (g & 1) * 16) = f32x4{z[0] * gf, z[1] * gf, z[2] * gf, z[3] * gf};
    }
    lds_arrive(SY + SY_C1, lane);
    STAMP(20);
#pragma unroll
    for (int t = 0; t < 5; ++t) {
      const int u = (j + 8 * t) * 64 + lane;
      if (u < 136 * 18) *(LAS v4u*)(L + L_WI + u * 16) = Wt[t];
    }
    lds_arrive(SY + SY_C2, lane);
    spin_lds(SY + SY_C2, 8);
    spin_lds(SY + SY_H, 1);
    s16x4 bo[2][4];
    f32x4 dm[2];
    {
      LAS unsigned char* wb = L + L_WI + (4 * g + (c >> 2)) * 288 + (c & 3) * 8;
      LAS unsigned char* ha = L + L_HC + (lane & 1) * 160 + g * 8;
#pragma unroll
      for (int t = 0; t < 2; ++t) {
        if (t == 0 || j == 0) {
          LAS unsigned char* wt = wb + (t == 0 ? j * 32 : 256);
#pragma unroll
          for (int k = 0; k < 4; ++k) bo[t][k] = trd(wt + k * 16 * 288);
          dm[t] = f32x4{0, 0, 0, 0};
#pragma unroll
          for (int k = 0; k < 4; ++k) dm[t] = mfma16(*(LAS s16x4*)(ha + k * 32), trd(wt + (64 + k * 16) * 288), dm[t]);
          dm[t] = mfma16(*(LAS s16x4*)(ha + 128), trd(wt + (64 + 56) * 288), dm[t]);
        }
      }
    }
    const float g0 = *(LAS float*)(L + L_G), g1 = *(LAS float*)(L + L_G + 4);
    const u32 tl = (u32)__builtin_amdgcn_readfirstlane((int)*(LAS u32*)(L + L_G + 8)), th = (u32)__builtin_amdgcn_readfirstlane((int)*(LAS u32*)(L + L_G + 12));
    const int kvc = rho >> 2, qt = rho & 3;
    STAMP(21);
#pragma nounroll
    for (u32 it = 0; it < CAP_GLB; ++it) {
      const v4u o = ldg(rs, H2_OFF + ((kvc * 16 + (j & 1) * 8 + (lane >> 3)) * 256 + 64 * qt + 16 * (j >> 1) + 2 * (lane & 7)) * 8);
      const v4u st = ldg(rs, ST_OFF + ((kvc * 16 + (j & 1) * 8 + (lane >> 3)) * 4 + (qt >> 1) * 2) * 8);
      if (__all((g_bad(o, tl, th) | g_bad(st, tl, th)) == 0u)) {
        const float mv = g_val(st.x);
        const float M = max16_32(fmaxf(mv, dpp<ROR8>(mv)));
        const float e = __builtin_amdgcn_exp2f(mv - M);
        float l = e * g_val(st.z); l = sum16_32(l + dpp<ROR8>(l));
        const float fs = e / l;
        float a0 = fs * g_val(o.x), a1 = fs * g_val(o.z);
        a0 += dpp<ROR8>(a0); a1 += dpp<ROR8>(a1); a0 = sum16_32(a0); a1 = sum16_32(a1);
        if (lane < 8) *(LAS u32*)(L + L_OC + (j & 1) * 128 + (16 * (j >> 1) + 2 * lane) * 2) = pk_bf16(a0, a1);
        break;
      }
      MEMBAR();
    }
    lds_arrive(SY + SY_C3, lane);
    STAMP(22);
    spin_lds_tight(SY + SY_C3, 8);
    {
      LAS unsigned char* oa = L + L_OC + (lane & 1) * 128 + g * 8;
      s16x4 ao[4];
#pragma unroll
      for (int k = 0; k < 4; ++k) ao[k] = *(LAS s16x4*)(oa + k * 32);
#pragma unroll
      for (int t = 0; t < 2; ++t) {
        if (t == 0 || j == 0) {
          f32x4 da = {0, 0, 0, 0};
#pragma unroll
          for (int k = 0; k < 4; ++k) da = mfma16(ao[k], bo[t][k], da);
          const float v0 = g0 * da[0] + (1.f - g0) * dm[t][0], v1 = g1 * da[1] + (1.f - g1) * dm[t][1];
          const int col = 144 * cb + (t == 0 ? 16 * j : 128) + c;
          if (lane < 16) stg(g_pack(v0, v1, tl, th), rs, H3_OFF + (((col >> 2) * 64 + rho) * 4 + (col & 3)) * 16);
        }
      }
    }
    STAMP(23);
    STAMP_FLUSH(16);
  }
}

extern "C" void kernel_launch(void* const* d_in, const int* in_sizes, int n_in, void* d_out, int out_size, void* d_ws, size_t ws_size, hipStream_t stream) {
  (void)in_sizes; (void)n_in; (void)out_size; (void)ws_size;
  (void)hipFuncSetAttribute((const void*)fused_decode, hipFuncAttributeMaxDynamicSharedMemorySize, L_END);
  fused_decode<<<NWG, NTHR, L_END, stream>>>((const u16*)d_in[0], (const u16*)d_in[1], (const u16*)d_in[2], (const u16*)d_in[3], (const u16*)d_in[4],
      (const u16*)d_in[6], (const u16*)d_in[7], (const u16*)d_in[8], (const u16*)d_in[9], (const u16*)d_in[10], (u16*)d_out, (unsigned char*)d_ws);
}
```

```cpp
#include <hip/hip_runtime.h>
#include <stdint.h>

typedef unsigned u32; typedef unsigned long long u64; typedef unsigned short u16;
typedef u32 v4u __attribute__((ext_vector_type(4)));
typedef u32 v2u __attribute__((ext_vector_type(2)));
typedef float f32x4 __attribute__((ext_vector_type(4)));
typedef float f32x2 __attribute__((ext_vector_type(2)));
typedef short s16x4 __attribute__((ext_vector_type(4)));
typedef short s16x8 __attribute__((ext_vector_type(8)));
typedef __bf16 bf16x8 __attribute__((ext_vector_type(8)));
typedef __bf16 bf16x2 __attribute__((ext_vector_type(2)));
#define GAS __attribute__((address_space(1)))
#define LAS __attribute__((address_space(3)))

#define ABL_NOW1 0
#define ABL_NOQ 0
#define ABL_NOH 0
#define KDELAY 0
#define QDELAY 0
#define ILV 0
#define INTF 1
#define ILV2 0
#define ROTM 6
#define ORDER 0
#define TAU 295
#define TT0 170
#define CAP 8
#define WMODE 0
#define EARLY 0
#define TOUCH 1
#define LATEQK 1
#define HDELAY 30
#define STAMPS 0

constexpr int D = 576, NQ = 4096, F = 4608, SK = 32768, WIN = 4096;
constexpr int NWG = 256, NTHR = 640;
constexpr u32 QP_OFF = 4096, HP_OFF = QP_OFF + 2 * 4 * NQ * 8, ST_OFF = HP_OFF + 3 * F * 16, H2_OFF = ST_OFF + 16 * 16 * 4 * 8,
              H3_OFF = H2_OFF + 16 * 16 * 256 * 8, WS_USED = H3_OFF + 144 * 64 * 4 * 16;
constexpr int L_K = 0, L_RED = 0, L_WI = 8192, L_WT = 131072  , L_XS = L_WT + 192 * 128, L_QS = L_XS + 2 * D * 2,
              L_WST = L_QS + 512, L_GF = L_WST + 128, L_HC = L_GF + 64, L_OC = L_HC + 2 * 80 * 2, L_FT = L_OC + 2 * 64 * 2, L_G = L_FT + 64,
              L_SY = L_G + 32, L_DUM = L_SY + 64, L_END = L_DUM + 256;
static_assert(L_END <= 163840 && L_WI + 136 * 288 <= 131072 && (L_HC % 16) == 0 && (L_OC % 16) == 0 && (L_QS % 16) == 0, "LDS map");
enum { SY_Q = 0, SY_C0, SY_G, SY_H, SY_C1, SY_C2, SY_F, SY_C3, SY_W, SY_QP, SY_W1, SY_T };
constexpr int OOB = 0x40000000;
constexpr u32 CAP_LDS = 1u << 20, CAP_GLB = 1u << 16;
constexpr float C2 = 0.08838834764831845f * 1.4426950408889634f;

__device__ __forceinline__ float asf(u32 x) { return __builtin_bit_cast(float, x); }
__device__ __forceinline__ u32 asu(float x) { return __builtin_bit_cast(u32, x); }
__device__ __forceinline__ u32 bf16r(float f) { u32 u = asu(f); return (u + 0x7FFFu + ((u >> 16) & 1u)) >> 16; }
__device__ __forceinline__ u32 pk_bf16(float lo, float hi) { return bf16r(lo) | (bf16r(hi) << 16); }
__device__ __forceinline__ float dot2(u32 a, u32 b, float c) { return __builtin_amdgcn_fdot2_f32_bf16(__builtin_bit_cast(bf16x2, a), __builtin_bit_cast(bf16x2, b), c, false); }
template <int CTRL> __device__ __forceinline__ float dpp(float x) { return asf((u32)__builtin_amdgcn_mov_dpp((int)asu(x), CTRL, 0xf, 0xf, true)); }
constexpr int XOR1 = 0xB1, XOR2 = 0x4E, HMIR = 0x141, ROR8 = 0x128, ROR4 = 0x124, ROR2 = 0x122, ROR1 = 0x121;
__device__ __forceinline__ float sum16(float x) { auto s = __builtin_amdgcn_permlane16_swap(asu(x), asu(x), false, false); return asf(s[0]) + asf(s[1]); }
__device__ __forceinline__ float sum32(float x) { auto t = __builtin_amdgcn_permlane32_swap(asu(x), asu(x), false, false); return asf(t[0]) + asf(t[1]); }
__device__ __forceinline__ float sum16_32(float x) { return sum32(sum16(x)); }
__device__ __forceinline__ float max16_32(float x) {
  auto s = __builtin_amdgcn_permlane16_swap(asu(x), asu(x), false, false); x = fmaxf(asf(s[0]), asf(s[1]));
  auto t = __builtin_amdgcn_permlane32_swap(asu(x), asu(x), false, false); return fmaxf(asf(t[0]), asf(t[1]));
}
__device__ __forceinline__ float wave_sum(float x) { x += dpp<ROR1>(x); x += dpp<ROR2>(x); x += dpp<ROR4>(x); x += dpp<ROR8>(x); return sum16_32(x); }
__device__ __forceinline__ float wave_max(float x) { x = fmaxf(x, dpp<ROR1>(x)); x = fmaxf(x, dpp<ROR2>(x)); x = fmaxf(x, dpp<ROR4>(x)); x = fmaxf(x, dpp<ROR8>(x)); return max16_32(x); }
__device__ __forceinline__ u32 g_lo(float v, u32 tl) { return ((asu(v) + 0x80u) >> 8) | tl; }
__device__ __forceinline__ float g_val(u32 lo) { return asf(lo << 8); }
__device__ __forceinline__ u32 g_bad(v4u q, u32 tl, u32 th) { return (((q.x ^ tl) | (q.z ^ tl)) & 0xFF000000u) | (q.y ^ th) | (q.w ^ th); }
__device__ __forceinline__ v4u g_pack(float a, float b, u32 tl, u32 th) { return v4u{g_lo(a, tl), th, g_lo(b, tl), th}; }
#define WAITV(n) asm volatile("s_waitcnt vmcnt(" #n ")" ::: "memory")
#define WAITL() asm volatile("s_waitcnt lgkmcnt(0)" ::: "memory")
#define MEMBAR() asm volatile("" ::: "memory")
#define SB() __builtin_amdgcn_sched_barrier(0)
__device__ __forceinline__ void spin_lds(LAS u32* p, u32 want) {
#pragma nounroll
  for (u32 n = 0; n < CAP_LDS; ++n) {
    if ((u32)__builtin_amdgcn_readfirstlane((int)*(volatile LAS u32*)p) >= want) break;
    __builtin_amdgcn_s_sleep(1);
  }
  MEMBAR();
}
__device__ __forceinline__ void spin_lds_tight(LAS u32* p, u32 want) {
#pragma nounroll
  for (u32 n = 0; n < CAP_LDS; ++n) { if ((u32)__builtin_amdgcn_readfirstlane((int)*(volatile LAS u32*)p) >= want) break; }
  MEMBAR();
}
__device__ __forceinline__ void lds_arrive(LAS u32* p, int lane) { MEMBAR(); if (lane == 0) __hip_atomic_fetch_add(p, 1u, __ATOMIC_RELAXED, __HIP_MEMORY_SCOPE_WORKGROUP); MEMBAR(); }
__device__ __forceinline__ void lds_set(LAS u32* p, int lane) { MEMBAR(); if (lane == 0) *(volatile LAS u32*)p = 1u; MEMBAR(); }
__device__ __forceinline__ s16x4 trd(LAS unsigned char* p) { return __builtin_amdgcn_ds_read_tr16_b64_v4i16((LAS s16x4*)p); }
__device__ __forceinline__ f32x4 mfma32v(v4u a, v4u b, f32x4 c) { return __builtin_amdgcn_mfma_f32_16x16x32_bf16(__builtin_bit_cast(bf16x8, a), __builtin_bit_cast(bf16x8, b), c, 0, 0, 0); }
__device__ __forceinline__ f32x4 mfma32(v4u a, s16x4 b0, s16x4 b1, f32x4 c) {
  s16x8 b = __builtin_shufflevector(b0, b1, 0, 1, 2, 3, 4, 5, 6, 7);
  return __builtin_amdgcn_mfma_f32_16x16x32_bf16(__builtin_bit_cast(bf16x8, a), __builtin_bit_cast(bf16x8, b), c, 0, 0, 0);
}
__device__ __forceinline__ f32x4 mfma16(s16x4 a, s16x4 b, f32x4 c) { return __builtin_amdgcn_mfma_f32_16x16x16bf16_1k(a, b, c, 0, 0, 0); }
__device__ __forceinline__ void glds_nt(const void* gsrc, u32 lds_dst) {
  u32 keep;
  asm volatile("s_mov_b32 %0, m0\n\ts_mov_b32 m0, %2\n\ts_nop 0\n\tglobal_load_lds_dwordx4 %1, off nt\n\ts_mov_b32 m0, %0" : "=&s"(keep) : "v"(gsrc), "s"(lds_dst) : "memory");
}
__device__ __forceinline__ void glds_nt_s(const void* sbase, u32 voff, u32 lds_dst) {
  u32 keep;
  asm volatile("s_mov_b32 %0, m0\n\ts_mov_b32 m0, %3\n\ts_nop 0\n\tglobal_load_lds_dwordx4 %1, %2 nt\n\ts_mov_b32 m0, %0" : "=&s"(keep) : "v"(voff), "s"(sbase), "s"(lds_dst) : "memory");
}
__device__ __forceinline__ void glds(const void* gsrc, u32 lds_dst) {
  u32 keep;
  asm volatile("s_mov_b32 %0, m0\n\ts_mov_b32 m0, %2\n\ts_nop 0\n\tglobal_load_lds_dwordx4 %1, off\n\ts_mov_b32 m0, %0" : "=&s"(keep) : "v"(gsrc), "s"(lds_dst) : "memory");
}
__device__ __forceinline__ v4u ldnt(const u16* base, u32 off) { return __builtin_nontemporal_load((const GAS v4u*)((const GAS char*)base + off)); }
__device__ __forceinline__ v4u ldg(__amdgpu_buffer_rsrc_t rs, int off) { return __builtin_amdgcn_raw_buffer_load_b128(rs, off, 0, 16); }
__device__ __forceinline__ void stg(v4u v, __amdgpu_buffer_rsrc_t rs, int off) { __builtin_amdgcn_raw_buffer_store_b128(v, rs, off, 0, 16); }
template <int N, int SLP> __device__ __forceinline__ void gather(__amdgpu_buffer_rsrc_t rs, const int (&off)[N], v4u (&v)[N], u32 tl, u32 th) {
  u32 need = (1u << N) - 1u;
#pragma unroll
  for (int n = 0; n < N; ++n) v[n] = v4u{0, 0, 0, 0};
#pragma nounroll
  for (u32 it = 0; it < CAP_GLB; ++it) {
    v4u t[N];
#pragma unroll
    for (int n = 0; n < N; ++n) t[n] = ldg(rs, ((need >> n) & 1u) ? off[n] : OOB);
#pragma unroll
    for (int n = 0; n < N; ++n) if (((need >> n) & 1u) && g_bad(t[n], tl, th) == 0u) { v[n] = t[n]; need &= ~(1u << n); }
    if (__all(need == 0u)) break;
    MEMBAR();
    if (SLP) __builtin_amdgcn_s_sleep(SLP);
  }
}
#define STAMP(i) do {} while (0)
#define STAMP_DECL do {} while (0)
#define STAMP_FLUSH(base) do {} while (0)

__global__ void __launch_bounds__(NTHR, 3) fused_decode(const u16* x, const u16* kc, const u16* vc, const u16* wq, const u16* wo, const u16* w1, const u16* b1,
    const u16* w2, const u16* b2, const u16* gw, u16* out, unsigned char* ws) {
  extern __shared__ __attribute__((aligned(16))) unsigned char lds_raw[];
  LAS unsigned char* L = (LAS unsigned char*)lds_raw;
  const u32 LB = (u32)(uintptr_t)L;
  LAS u32* SY = (LAS u32*)(L + L_SY);
  asm volatile("" :: "s"(x), "s"(kc), "s"(vc), "s"(wq), "s"(wo), "s"(w1), "s"(b1), "s"(w2), "s"(b2), "s"(gw), "s"(out), "s"(ws));
  const u32 tstart = (u32)__builtin_amdgcn_s_memrealtime();
  const int w = blockIdx.x, lane = threadIdx.x & 63, wave = __builtin_amdgcn_readfirstlane((int)(threadIdx.x >> 6));
  const int bh = w >> 3, s = w & 7,
    b = bh >> 4, kvh = bh & 15, m = b * 8 + s;
  const int phi = (w & 7) * 32 + (w >> 3);
  const int cbk = phi & 63, rb = phi >> 6;
  const int cb1 = w % 72, rb1 = w / 72;
  const int rho = phi >> 2, cb = phi & 3;
  const int g = lane >> 4, c = lane & 15;
  const __amdgpu_buffer_rsrc_t rs = __builtin_amdgcn_make_buffer_rsrc(ws, 0, WS_USED, 0x00020000);
  if (wave < 9) {
    const int r0 = 16 * wave + (lane >> 3);
    const u16* src = wq + (size_t)(144 * rb + r0) * NQ + 64 * cbk;
    const int sw0 = (r0 & 2) | ((r0 >> 1) & 4), sw1 = ((r0 + 8) & 2) | (((r0 + 8) >> 1) & 4);
    glds_nt(src + ((lane & 7) ^ sw0) * 8, LB + L_WT + wave * 2048);
    glds_nt(src + 8 * NQ + ((lane & 7) ^ sw1) * 8, LB + L_WT + wave * 2048 + 1024);
  }

  if (wave == 0) {
    __builtin_amdgcn_s_setprio(3);
    STAMP_DECL;
    if (lane < 16) ((volatile LAS u32*)SY)[lane] = 0u;
    u64 E;
    asm volatile("global_load_dwordx2 %0, %1, off sc1" : "=v"(E) : "v"(ws) : "memory");
    glds(x + lane * 8, LB + L_XS);
    glds(x + 512 + lane * 8, LB + L_XS + 1024);
    if (lane < 16) glds(x + 1024 + lane * 8, LB + L_XS + 2048);
    {
      const u16* vw = vc + ((size_t)bh * SK + (SK - WIN) + s * 512) * 128;
      const int l8 = TOUCH == 1 ? 0 : (lane & 7);
      const u16* p = l8 == 0 ? vw : l8 == 1 ? w1 + (size_t)(192 * (rb1 % 3)) * F + 64 * cb1 : l8 == 2 ? w1 + (size_t)(192 * (rb1 % 3) + 191) * F + 64 * cb1
                   : l8 == 3 ? wo + (size_t)(64 * rho) * D + 144 * cb : l8 == 4 ? wo + (size_t)(64 * rho + 63) * D + 144 * cb
                   : l8 == 5 ? w2 + (size_t)(72 * rho) * D + 144 * cb : l8 == 6 ? w2 + (size_t)(72 * rho + 71) * D + 144 * cb : vw + 65534;
      u32 keep;
      asm volatile("s_mov_b32 %0, m0\n\ts_mov_b32 m0, %2\n\ts_nop 0\n\tglobal_load_lds_dword %1, off nt\n\ts_mov_b32 m0, %0" : "=&s"(keep) : "v"(p), "s"(LB + L_DUM) : "memory");
    }
    WAITL();
    __builtin_amdgcn_s_barrier();
    asm volatile("s_waitcnt vmcnt(1)" : "+v"(E) :: "memory");
    const u64 T = (E + 1ull) & 0xFFFFFFFFFFull;
    const u32 tl = (u32)__builtin_amdgcn_readfirstlane((int)((u32)(T & 0xFFu) << 24)), th = (u32)__builtin_amdgcn_readfirstlane((int)(u32)(T >> 8));
    if (lane == 0) { *(LAS u32*)(L + L_G + 8) = tl; *(LAS u32*)(L + L_G + 12) = th; }
    lds_set(SY + SY_T, lane);
    STAMP(1);
    spin_lds_tight(SY + SY_W, 8);
    STAMP(2);
    {
      f32x4 d[4];
      LAS unsigned char* xa = L + L_XS + (lane & 1) * (D * 2) + rb * 288;
      const int rq = 8 * g + (c >> 2);
#pragma unroll
      for (int t = 0; t < 4; ++t) d[t] = f32x4{0, 0, 0, 0};
#pragma unroll
      for (int ks = 0; ks < 4; ++ks) {
        const v4u a = *(LAS v4u*)(xa + ks * 64 + g * 16);
        const int r0 = 32 * ks + rq, r1 = r0 + 4;
        const int s0 = (r0 & 2) | ((r0 >> 1) & 4), s1 = (r1 & 2) | ((r1 >> 1) & 4);
#pragma unroll
        for (int t = 0; t < 4; ++t) {
          const int ch = 2 * t + ((c & 3) >> 1);
          d[t] = mfma32(a, trd(L + L_WT + r0 * 128 + (ch ^ s0) * 16 + (c & 1) * 8), trd(L + L_WT + r1 * 128 + (ch ^ s1) * 16 + (c & 1) * 8), d[t]);
        }
      }
      {
        const s16x4 a = *(LAS s16x4*)(xa + 256 + g * 8);
        const int r0 = 128 + 4 * g + (c >> 2), s0 = (r0 & 2) | ((r0 >> 1) & 4);
#pragma unroll
        for (int t = 0; t < 4; ++t) d[t] = mfma16(a, trd(L + L_WT + r0 * 128 + ((2 * t + ((c & 3) >> 1)) ^ s0) * 16 + (c & 1) * 8), d[t]);
      }
      const float v0 = g == 0 ? d[0][0] : g == 1 ? d[1][0] : g == 2 ? d[2][0] : d[3][0], v1 = g == 0 ? d[0][1] : g == 1 ? d[1][1] : g == 2 ? d[2][1] : d[3][1];
      const float rv = dpp<XOR1>((c & 1) ? v0 : v1);
      stg((c & 1) ? g_pack(rv, v1, tl, th) : g_pack(v0, rv, tl, th), rs, QP_OFF + (((c & 1) * 4 + rb) * NQ + 64 * cbk + 16 * g + (c & ~1)) * 8);
    }
    STAMP(3);
    WAITL();
    lds_set(SY + SY_QP, lane);
    if (QDELAY) __builtin_amdgcn_s_sleep(QDELAY);
    {
      int off[8]; v4u q[8];
#pragma unroll
      for (int n = 0; n < 8; ++n) off[n] = QP_OFF + (b * 4 * NQ + kvh * 256 + 2 * lane) * 8 + (n >> 1) * NQ * 8 + (n & 1) * 1024;
      gather<8, 4>(rs, off, q, tl, th);
#pragma unroll
      for (int h = 0; h < 2; ++h) {
        const float lo = (g_val(q[h].x) + g_val(q[2 + h].x)) + (g_val(q[4 + h].x) + g_val(q[6 + h].x)), hi = (g_val(q[h].z) + g_val(q[2 + h].z)) + (g_val(q[4 + h].z) + g_val(q[6 + h].z));
        *(LAS u32*)(L + L_QS + h * 256 + lane * 4) = pk_bf16(lo, hi);
      }
    }
    lds_set(SY + SY_Q, lane);
    STAMP(4);
    spin_lds(SY + SY_C0, 8);
    {
      const int jj = lane & 7, hh = (lane >> 3) & 1;
      const float mw = *(LAS float*)(L + L_WST + jj * 16 + hh * 8), lw = *(LAS float*)(L + L_WST + jj * 16 + hh * 8 + 4);
      float mm = fmaxf(mw, dpp<XOR1>(mw)); mm = fmaxf(mm, dpp<XOR2>(mm)); mm = fmaxf(mm, dpp<HMIR>(mm));
      mm = g_val(g_lo(mm, 0u));
      const float gf = __builtin_amdgcn_exp2f(mw - mm);
      float ll = gf * lw; ll += dpp<XOR1>(ll); ll += dpp<XOR2>(ll); ll += dpp<HMIR>(ll);
      if (lane < 16) *(LAS float*)(L + L_GF + jj * 8 + hh * 4) = gf;
      lds_set(SY + SY_G, lane);
      if (lane < 16 && jj == 0) stg(g_pack(mm, ll, tl, th), rs, ST_OFF + ((kvh * 16 + m) * 4 + hh * 2) * 8);
    }
    STAMP(6);
    float b2v = 0.f;
    if (w < 144) b2v = asf((u32)b2[4 * w + (lane & 3)] << 16);
#define H2_PUBLISH() do { \
      f32x2 oa = *(LAS f32x2*)(L + L_RED + lane * 8), ob = *(LAS f32x2*)(L + L_RED + 512 + lane * 8); \
      _Pragma("unroll") for (int j = 1; j < 8; ++j) { oa += *(LAS f32x2*)(L + L_RED + j * 1024 + lane * 8); ob += *(LAS f32x2*)(L + L_RED + j * 1024 + 512 + lane * 8); } \
      const int off = H2_OFF + ((kvh * 16 + m) * 256 + lane * 2) * 8; \
      stg(g_pack(oa[0], oa[1], tl, th), rs, off); stg(g_pack(ob[0], ob[1], tl, th), rs, off + 1024); STAMP(11); } while (0)
    spin_lds_tight(SY + SY_C1, 8);
    H2_PUBLISH();
    STAMP(9);
    if (w < 144) {
      spin_lds(SY + SY_H, 1);
      int off[4]; v4u a[4];
#pragma unroll
      for (int n = 0; n < 4; ++n) off[n] = H3_OFF + (w * 256 + lane) * 16 + n * 1024;
      gather<4, 0>(rs, off, a, tl, th);
      float r0 = (g_val(a[0].x) + g_val(a[1].x)) + (g_val(a[2].x) + g_val(a[3].x)), r1 = (g_val(a[0].z) + g_val(a[1].z)) + (g_val(a[2].z) + g_val(a[3].z));
      r0 += dpp<ROR4>(r0); r0 += dpp<ROR8>(r0); r0 = sum16_32(r0);
      r1 += dpp<ROR4>(r1); r1 += dpp<ROR8>(r1); r1 = sum16_32(r1);
      if (lane < 8) {
        const int bb = lane >> 2;
        const float v = (bb ? r1 : r0) + (1.f - *(LAS float*)(L + L_G + bb * 4)) * b2v;
        out[bb * D + 4 * w + (lane & 3)] = (u16)bf16r(v);
      }
      if (w == 0 && lane == 0) __hip_atomic_store((GAS u64*)ws, E + 1ull, __ATOMIC_RELAXED, __HIP_MEMORY_SCOPE_AGENT);
      STAMP(12);
    }
    STAMP_FLUSH(0);
  } else if (wave == 9) {
    __builtin_amdgcn_s_setprio(2);
    STAMP_DECL;
    __builtin_amdgcn_s_barrier();
    u32 gwv[5];
#pragma unroll
    for (int n = 0; n < 5; ++n) gwv[n] = ((const u32*)gw)[min(n * 64 + lane, 287)];
    const u32 b1a = b1[72 * rho + lane], b1b = b1[72 * rho + 64 + (lane & 7)];
    spin_lds(SY + SY_T, 1);
    const u32 tl = (u32)__builtin_amdgcn_readfirstlane((int)*(LAS u32*)(L + L_G + 8)), th = (u32)__builtin_amdgcn_readfirstlane((int)*(LAS u32*)(L + L_G + 12));
    {
      float s0 = 0.f, s1 = 0.f;
#pragma unroll
      for (int n = 0; n < 5; ++n) { const int i = n * 64 + lane; if (i < 288) { s0 = dot2(*(LAS u32*)(L + L_XS + i * 4), gwv[n], s0); s1 = dot2(*(LAS u32*)(L + L_XS + D * 2 + i * 4), gwv[n], s1); } }
      s0 = wave_sum(s0); s1 = wave_sum(s1);
      const float g0 = 1.f / (1.f + __expf(-s0)), g1 = 1.f / (1.f + __expf(-s1));
      if (lane == 0) { *(LAS float*)(L + L_G) = g0; *(LAS float*)(L + L_G + 4) = g1; }
    }
    if (w < 216) {
      spin_lds(SY + SY_W1, 8);
      f32x4 d[4];
      LAS unsigned char* xa = L + L_XS + (lane & 1) * (D * 2) + rb1 * 384;
      const int rq = 8 * g + (c >> 2);
#pragma unroll
      for (int t = 0; t < 4; ++t) d[t] = f32x4{0, 0, 0, 0};
#pragma unroll
      for (int ks = 0; ks < 6; ++ks) {
        const v4u a = *(LAS v4u*)(xa + ks * 64 + g * 16);
        const int r0 = 32 * ks + rq, r1 = r0 + 4;
        const int s0 = (r0 & 2) | ((r0 >> 1) & 4), s1 = (r1 & 2) | ((r1 >> 1) & 4);
#pragma unroll
        for (int t = 0; t < 4; ++t) {
          const int ch = 2 * t + ((c & 3) >> 1);
          d[t] = mfma32(a, trd(L + L_WT + r0 * 128 + (ch ^ s0) * 16 + (c & 1) * 8), trd(L + L_WT + r1 * 128 + (ch ^ s1) * 16 + (c & 1) * 8), d[t]);
        }
      }
      const float v0 = g == 0 ? d[0][0] : g == 1 ? d[1][0] : g == 2 ? d[2][0] : d[3][0], v1 = g == 0 ? d[0][1] : g == 1 ? d[1][1] : g == 2 ? d[2][1] : d[3][1];
      stg(g_pack(v0, v1, tl, th), rs, HP_OFF + (rb1 * F + 64 * cb1 + lane) * 16);
    }
    STAMP(5);
    if (HDELAY) __builtin_amdgcn_s_sleep(HDELAY);
    {
      int off[6]; v4u h[6];
#pragma unroll
      for (int n = 0; n < 3; ++n) { off[n] = HP_OFF + (n * F + 72 * rho + lane) * 16; off[3 + n] = HP_OFF + (n * F + 72 * rho + 64 + (lane & 7)) * 16; }
      gather<6, 4>(rs, off, h, tl, th);
      const float pa0 = (g_val(h[0].x) + g_val(h[1].x)) + g_val(h[2].x), pa1 = (g_val(h[0].z) + g_val(h[1].z)) + g_val(h[2].z);
      const float pb0 = (g_val(h[3].x) + g_val(h[4].x)) + g_val(h[5].x), pb1 = (g_val(h[3].z) + g_val(h[4].z)) + g_val(h[5].z);
      auto act = [](float p, u32 bias) { float v = asf(bf16r(asf(bf16r(p) << 16) + asf(bias << 16)) << 16); v = fmaxf(v, 0.f); return (u16)bf16r(v * v); };
      *(LAS u16*)(L + L_HC + lane * 2) = act(pa0, b1a); *(LAS u16*)(L + L_HC + 160 + lane * 2) = act(pa1, b1a);
      if (lane < 8) { *(LAS u16*)(L + L_HC + (64 + lane) * 2) = 0; *(LAS u16*)(L + L_HC + 160 + (64 + lane) * 2) = 0;
                      *(LAS u16*)(L + L_HC + (72 + lane) * 2) = act(pb0, b1b); *(LAS u16*)(L + L_HC + 160 + (72 + lane) * 2) = act(pb1, b1b); }
      lds_set(SY + SY_H, lane);
    }
    STAMP(10);
    STAMP_FLUSH(0);
  } else {
    const int j = wave - 1;
    STAMP_DECL;
    const size_t kvoff = ((size_t)bh * SK + (SK - WIN) + s * 512) * 128;
    const u16* kb = kc + kvoff;
    const u16* vb = vc + kvoff;
    const u32 lin = j * 1024 + lane * 16;
    const __amdgpu_buffer_rsrc_t rv = __builtin_amdgcn_make_buffer_rsrc((void*)vb, 0, 131072, 0x00020000);
    const int rot = ROTM == 0 ? 0 : ROTM == 1 ? bh : ROTM == 2 ? bh + 8 * b : ROTM == 3 ? 3 * bh : ROTM == 4 ? bh + 4 * b : ROTM == 5 ? 5 * bh + 8 * b : ROTM == 6 ? bh + (s >> 2) * 8 : bh;
    const int dir = (ROTM == 7 && b) ? -1 : 1;
#define KLOAD(n) glds_nt_s(kb + ((dir * (n) + rot) & 15) * 4096, j * 1024 + g * 256 + ((c ^ (n)) * 16), LB + L_K + j * 16384 + (n) * 1024)
#define VLOAD(n) V[n] = __builtin_amdgcn_raw_buffer_load_b128(rv, lin, ((dir * (n) + rot) & 15) * 8192, 2)
    v4u V[16], Wt[5];
#define W1LOAD(p) do { const int r_ = 8 * (3 * j + (p)) + (lane >> 3); \
      glds_nt(w1 + (size_t)(192 * rb1 + r_) * F + 64 * cb1 + ((lane & 7) ^ ((r_ & 2) | ((r_ >> 1) & 4))) * 8, LB + L_WT + (3 * j + (p)) * 1024); } while (0)
#define WAITC() WAITV(7)
    int slot_k = 0;
#define S_(X) do { WAITC(); { const u32 due_ = TT0 + (u32)(slot_k * TAU) / 10u; ++slot_k; \
      _Pragma("nounroll") for (int it_ = 0; it_ < 4096 && (u32)__builtin_amdgcn_s_memrealtime() - tstart < due_; ++it_) __builtin_amdgcn_s_sleep(1); } X; SB(); } while (0)
    KLOAD(0); KLOAD(1); KLOAD(2); KLOAD(3);
    SB();
    __builtin_amdgcn_s_barrier();
    WAITV(4);
    lds_arrive(SY + SY_W, lane);
    S_(KLOAD(4)); S_(KLOAD(5)); S_(KLOAD(6)); S_(KLOAD(7)); S_(KLOAD(8)); S_(KLOAD(9));
    if (w < 216) {
      spin_lds(SY + SY_QP, 1);
      S_(W1LOAD(0)); S_(W1LOAD(1)); S_(W1LOAD(2));
      S_(KLOAD(10)); S_(KLOAD(11)); S_(KLOAD(12)); S_(KLOAD(13));
    } else {
      slot_k += 3;
      S_(KLOAD(10)); S_(KLOAD(11)); S_(KLOAD(12)); S_(KLOAD(13));
    }
    S_(KLOAD(14)); S_(KLOAD(15));
    S_(VLOAD(0)); S_(VLOAD(1));
    WAITV(8);
    lds_arrive(SY + SY_W1, lane);
    S_(VLOAD(2)); S_(VLOAD(3));
    S_(VLOAD(4)); S_(VLOAD(5)); S_(VLOAD(6)); S_(VLOAD(7));
    WAITV(8);
    STAMP(16);
    spin_lds(SY + SY_Q, 1);
    STAMP(17);
    f32x4 d[4];
#pragma unroll
    for (int t = 0; t < 4; ++t) d[t] = f32x4{0, 0, 0, 0};
#define QK(u) do { const v4u a_ = *(LAS v4u*)(L + L_QS + (c & 1) * 256 + (u) * 64 + g * 16); \
      _Pragma("unroll") for (int t = 0; t < 4; ++t) d[t] = mfma32v(a_, *(LAS v4u*)(L + L_K + j * 16384 + c * 1024 + t * 256 + (((4 * (u) + g) ^ c) * 16)), d[t]); \
      asm volatile("" : "+v"(d[0]), "+v"(d[1]), "+v"(d[2]), "+v"(d[3])); SB(); } while (0)
    S_(VLOAD(8)); QK(0); QK(1);
    S_(VLOAD(9)); QK(2); QK(3);
    S_(VLOAD(10));
    const float s0 = (g == 0 ? d[0][0] : g == 1 ? d[1][0] : g == 2 ? d[2][0] : d[3][0]) * C2;
    const float s1 = (g == 0 ? d[0][1] : g == 1 ? d[1][1] : g == 2 ? d[2][1] : d[3][1]) * C2;
    float m0 = wave_max(s0), m1 = wave_max(s1);
    asm volatile("" : "+v"(m0), "+v"(m1)); SB();
    S_(VLOAD(11));
    float p0 = __builtin_amdgcn_exp2f(s0 - m0), p1 = __builtin_amdgcn_exp2f(s1 - m1);
    const float l0 = wave_sum(p0), l1 = wave_sum(p1);
    if (lane == 0) *(LAS f32x4*)(L + L_WST + j * 16) = f32x4{m0, l0, m1, l1};
    lds_arrive(SY + SY_C0, lane);
    asm volatile("" : "+v"(p0), "+v"(p1)); SB();
    STAMP(18);
    f32x2 acc[2][4];
#pragma unroll
    for (int k = 0; k < 4; ++k) { acc[0][k] = f32x2{0.f, 0.f}; acc[1][k] = f32x2{0.f, 0.f}; }
    const u16* wop = wo + (size_t)(64 * rho) * D + 144 * cb;
    const u16* w2p = w2 + (size_t)(72 * rho) * D + 144 * cb - 64 * D;
#define WLOAD(t) do { const int u_ = min((j + 8 * (t)) * 64 + lane, 136 * 18 - 1), r_ = u_ / 18; \
      Wt[t] = __builtin_nontemporal_load((const GAS v4u*)((r_ < 64 ? wop : w2p) + r_ * D + (u_ - r_ * 18) * 8)); } while (0)
#define PV(n) do { const float a_ = dpp<0x150 + (n)>(p0), b_ = dpp<0x150 + (n)>(p1); \
      _Pragma("unroll") for (int k = 0; k < 4; ++k) { const f32x2 v_ = {asf(V[n][k] << 16), asf(V[n][k] & 0xFFFF0000u)}; \
        acc[0][k] = __builtin_elementwise_fma(v_, f32x2{a_, a_}, acc[0][k]); acc[1][k] = __builtin_elementwise_fma(v_, f32x2{b_, b_}, acc[1][k]); } \
      asm volatile("" : "+v"(acc[0][0]), "+v"(acc[0][1]), "+v"(acc[0][2]), "+v"(acc[0][3]), "+v"(acc[1][0]), "+v"(acc[1][1]), "+v"(acc[1][2]), "+v"(acc[1][3])); SB(); } while (0)
    S_(VLOAD(12)); PV(0); PV(1); PV(2); PV(3);
    S_(VLOAD(13)); PV(4); PV(5); PV(6); PV(7);
    S_(VLOAD(14)); PV(8); PV(9); PV(10);
    S_(VLOAD(15)); PV(11);
    S_(WLOAD(0)); PV(12);
    S_(WLOAD(1)); PV(13);
    S_(WLOAD(2)); PV(14);
    spin_lds(SY + SY_G, 1);
    const float gf = *(LAS float*)(L + L_GF + j * 8 + (g >> 1) * 4);
    SB();
    S_(WLOAD(3)); WLOAD(4); SB(); PV(15);
    STAMP(19);
    {
      float y[8], z[4];
#pragma unroll
      for (int k = 0; k < 8; ++k) { auto t = __builtin_amdgcn_permlane32_swap(asu(acc[0][k >> 1][k & 1]), asu(acc[1][k >> 1][k & 1]), false, false); y[k] = asf(t[0]) + asf(t[1]); }
#pragma unroll
      for (int k = 0; k < 4; ++k) { auto t = __builtin_amdgcn_permlane16_swap(asu(y[k]), asu(y[k + 4]), false, false); z[k] = asf(t[0]) + asf(t[1]); }
      *(LAS f32x4*)(L + L_RED + j * 1024 + (g >> 1) * 512 + c * 32 + (g & 1) * 16) = f32x4{z[0] * gf, z[1] * gf, z[2] * gf, z[3] * gf};
    }
    lds_arrive(SY + SY_C1, lane);
    STAMP(20);
    const int kvc = rho >> 2, qt = rho & 3;
    const u32 tl = (u32)__builtin_amdgcn_readfirstlane((int)*(LAS u32*)(L + L_G + 8)), th = (u32)__builtin_amdgcn_readfirstlane((int)*(LAS u32*)(L + L_G + 12));
    const int h2off = (int)(H2_OFF + ((kvc * 16 + (j & 1) * 8 + (lane >> 3)) * 256 + 64 * qt + 16 * (j >> 1) + 2 * (lane & 7)) * 8);
    const int stoff = (int)(ST_OFF + ((kvc * 16 + (j & 1) * 8 + (lane >> 3)) * 4 + (qt >> 1) * 2) * 8);
#pragma unroll
    for (int t = 0; t < 5; ++t) {
      const int u = (j + 8 * t) * 64 + lane;
      if (u < 136 * 18) *(LAS v4u*)(L + L_WI + u * 16) = Wt[t];
    }
    lds_arrive(SY + SY_C2, lane);
    STAMP(24);
    spin_lds(SY + SY_C2, 8);
    STAMP(25);
    spin_lds(SY + SY_H, 1);
    STAMP(26);
    s16x4 bo[2][4];
    f32x4 dm[2];
    {
      LAS unsigned char* wb = L + L_WI + (4 * g + (c >> 2)) * 288 + (c & 3) * 8;
      LAS unsigned char* ha = L + L_HC + (lane & 1) * 160 + g * 8;
#pragma unroll
      for (int t = 0; t < 2; ++t) {
        if (t == 0 || j == 0) {
          LAS unsigned char* wt = wb + (t == 0 ? j * 32 : 256);
#pragma unroll
          for (int k = 0; k < 4; ++k) bo[t][k] = trd(wt + k * 16 * 288);
          dm[t] = f32x4{0, 0, 0, 0};
#pragma unroll
          for (int k = 0; k < 4; ++k) dm[t] = mfma16(*(LAS s16x4*)(ha + k * 32), trd(wt + (64 + k * 16) * 288), dm[t]);
          dm[t] = mfma16(*(LAS s16x4*)(ha + 128), trd(wt + (64 + 56) * 288), dm[t]);
        }
      }
    }
    const float g0 = *(LAS float*)(L + L_G), g1 = *(LAS float*)(L + L_G + 4);
    STAMP(21);
    v4u o = ldg(rs, h2off), st = ldg(rs, stoff);
#pragma nounroll
    for (u32 it = 0; it < CAP_GLB && !__all((g_bad(o, tl, th) | g_bad(st, tl, th)) == 0u); ++it) { o = ldg(rs, h2off); st = ldg(rs, stoff); MEMBAR(); }
    {
      const float mv = g_val(st.x);
      const float M = max16_32(fmaxf(mv, dpp<ROR8>(mv)));
      const float e = __builtin_amdgcn_exp2f(mv - M);
      float l = e * g_val(st.z); l = sum16_32(l + dpp<ROR8>(l));
      const float fs = e / l;
      float a0 = fs * g_val(o.x), a1 = fs * g_val(o.z);
      a0 += dpp<ROR8>(a0); a1 += dpp<ROR8>(a1); a0 = sum16_32(a0); a1 = sum16_32(a1);
      if (lane < 8) *(LAS u32*)(L + L_OC + (j & 1) * 128 + (16 * (j >> 1) + 2 * lane) * 2) = pk_bf16(a0, a1);
    }
    lds_arrive(SY + SY_C3, lane);
    STAMP(22);
    spin_lds_tight(SY + SY_C3, 8);
    {
      LAS unsigned char* oa = L + L_OC + (lane & 1) * 128 + g * 8;
      s16x4 ao[4];
#pragma unroll
      for (int k = 0; k < 4; ++k) ao[k] = *(LAS s16x4*)(oa + k * 32);
#pragma unroll
      for (int t = 0; t < 2; ++t) {
        if (t == 0 || j == 0) {
          f32x4 da = {0, 0, 0, 0};
#pragma unroll
          for (int k = 0; k < 4; ++k) da = mfma16(ao[k], bo[t][k], da);
          const float v0 = g0 * da[0] + (1.f - g0) * dm[t][0], v1 = g1 * da[1] + (1.f - g1) * dm[t][1];
          const int col = 144 * cb + (t == 0 ? 16 * j : 128) + c;
          if (lane < 16) stg(g_pack(v0, v1, tl, th), rs, H3_OFF + (((col >> 2) * 64 + rho) * 4 + (col & 3)) * 16);
        }
      }
    }
    STAMP(23);
    STAMP_FLUSH(16);
  }
}

extern "C" void kernel_launch(void* const* d_in, const int* in_sizes, int n_in, void* d_out, int out_size, void* d_ws, size_t ws_size, hipStream_t stream) {
  (void)in_sizes; (void)n_in; (void)out_size; (void)ws_size;
  (void)hipFuncSetAttribute((const void*)fused_decode, hipFuncAttributeMaxDynamicSharedMemorySize, L_END);
  fused_decode<<<NWG, NTHR, L_END, stream>>>((const u16*)d_in[0], (const u16*)d_in[1], (const u16*)d_in[2], (const u16*)d_in[3], (const u16*)d_in[4],
      (const u16*)d_in[6], (const u16*)d_in[7], (const u16*)d_in[8], (const u16*)d_in[9], (const u16*)d_in[10], (u16*)d_out, (unsigned char*)d_ws);
}
```

```cpp
#include <hip/hip_runtime.h>
#include <stdint.h>

typedef unsigned u32; typedef unsigned long long u64; typedef unsigned short u16;
typedef u32 v4u __attribute__((ext_vector_type(4)));
typedef float f32x4 __attribute__((ext_vector_type(4)));
typedef float f32x2 __attribute__((ext_vector_type(2)));
typedef short s16x4 __attribute__((ext_vector_type(4)));
typedef short s16x8 __attribute__((ext_vector_type(8)));
typedef __bf16 bf16x8 __attribute__((ext_vector_type(8)));
typedef __bf16 bf16x2 __attribute__((ext_vector_type(2)));
#define GAS __attribute__((address_space(1)))
#define LAS __attribute__((address_space(3)))

constexpr int D = 576, NQ = 4096, F = 4608, SK = 32768, WIN = 4096;
constexpr int NWG = 256, NTHR = 640;
constexpr u32 T_SLOT0 = 170, SLOT_NS = 295, T_Q = 540, T_H = 1000, T_STAGGER = 80;
constexpr u32 QP_OFF = 4096, HP_OFF = QP_OFF + 2 * 4 * NQ * 8, ST_OFF = HP_OFF + 3 * F * 16, H2_OFF = ST_OFF + 16 * 16 * 4 * 8,
              H3_OFF = H2_OFF + 16 * 16 * 256 * 8, WS_USED = H3_OFF + 144 * 64 * 4 * 16;
constexpr int L_K = 0, L_RED = 0, L_WI = 8192, L_WT = 131072  , L_XS = L_WT + 192 * 128, L_QS = L_XS + 2 * D * 2,
              L_WST = L_QS + 512, L_GF = L_WST + 128, L_HC = L_GF + 64, L_OC = L_HC + 2 * 80 * 2, L_G = L_OC + 2 * 64 * 2,
              L_SY = L_G + 32, L_DUM = L_SY + 64, L_END = L_DUM + 256;
static_assert(L_END <= 163840 && L_WI + 136 * 288 <= 131072 && (L_HC % 16) == 0 && (L_OC % 16) == 0 && (L_QS % 16) == 0, "LDS map");
enum { SY_Q = 0, SY_C0, SY_G, SY_H, SY_C1, SY_C2, SY_C3, SY_W, SY_QP, SY_W1, SY_T };
constexpr int OOB = 0x40000000;
constexpr u32 CAP_LDS = 1u << 20, CAP_GLB = 1u << 16;
constexpr float C2 = 0.08838834764831845f * 1.4426950408889634f;

__device__ __forceinline__ float asf(u32 x) { return __builtin_bit_cast(float, x); }
__device__ __forceinline__ u32 asu(float x) { return __builtin_bit_cast(u32, x); }
__device__ __forceinline__ u32 bf16r(float f) { u32 u = asu(f); return (u + 0x7FFFu + ((u >> 16) & 1u)) >> 16; }
__device__ __forceinline__ u32 pk_bf16(float lo, float hi) { return bf16r(lo) | (bf16r(hi) << 16); }
__device__ __forceinline__ float dot2(u32 a, u32 b, float c) { return __builtin_amdgcn_fdot2_f32_bf16(__builtin_bit_cast(bf16x2, a), __builtin_bit_cast(bf16x2, b), c, false); }
template <int CTRL> __device__ __forceinline__ float dpp(float x) { return asf((u32)__builtin_amdgcn_mov_dpp((int)asu(x), CTRL, 0xf, 0xf, true)); }
constexpr int XOR1 = 0xB1, XOR2 = 0x4E, HMIR = 0x141, ROR8 = 0x128, ROR4 = 0x124, ROR2 = 0x122, ROR1 = 0x121;
__device__ __forceinline__ float sum16_32(float x) {
  auto s = __builtin_amdgcn_permlane16_swap(asu(x), asu(x), false, false); x = asf(s[0]) + asf(s[1]);
  auto t = __builtin_amdgcn_permlane32_swap(asu(x), asu(x), false, false); return asf(t[0]) + asf(t[1]);
}
__device__ __forceinline__ float max16_32(float x) {
  auto s = __builtin_amdgcn_permlane16_swap(asu(x), asu(x), false, false); x = fmaxf(asf(s[0]), asf(s[1]));
  auto t = __builtin_amdgcn_permlane32_swap(asu(x), asu(x), false, false); return fmaxf(asf(t[0]), asf(t[1]));
}
__device__ __forceinline__ float wave_sum(float x) { x += dpp<ROR1>(x); x += dpp<ROR2>(x); x += dpp<ROR4>(x); x += dpp<ROR8>(x); return sum16_32(x); }
__device__ __forceinline__ float wave_max(float x) { x = fmaxf(x, dpp<ROR1>(x)); x = fmaxf(x, dpp<ROR2>(x)); x = fmaxf(x, dpp<ROR4>(x)); x = fmaxf(x, dpp<ROR8>(x)); return max16_32(x); }
__device__ __forceinline__ u32 g_lo(float v, u32 tl) { return ((asu(v) + 0x80u) >> 8) | tl; }
__device__ __forceinline__ float g_val(u32 lo) { return asf(lo << 8); }
__device__ __forceinline__ u32 g_bad(v4u q, u32 tl, u32 th) { return (((q.x ^ tl) | (q.z ^ tl)) & 0xFF000000u) | (q.y ^ th) | (q.w ^ th); }
__device__ __forceinline__ v4u g_pack(float a, float b, u32 tl, u32 th) { return v4u{g_lo(a, tl), th, g_lo(b, tl), th}; }
#define WAITV(n) asm volatile("s_waitcnt vmcnt(" #n ")" ::: "memory")
#define WAITL() asm volatile("s_waitcnt lgkmcnt(0)" ::: "memory")
#define MEMBAR() asm volatile("" ::: "memory")
#define SB() __builtin_amdgcn_sched_barrier(0)
__device__ __forceinline__ void spin_lds(LAS u32* p, u32 want) {
#pragma nounroll
  for (u32 n = 0; n < CAP_LDS; ++n) {
    if ((u32)__builtin_amdgcn_readfirstlane((int)*(volatile LAS u32*)p) >= want) break;
    __builtin_amdgcn_s_sleep(1);
  }
  MEMBAR();
}
__device__ __forceinline__ void spin_lds_tight(LAS u32* p, u32 want) {
#pragma nounroll
  for (u32 n = 0; n < CAP_LDS; ++n) { if ((u32)__builtin_amdgcn_readfirstlane((int)*(volatile LAS u32*)p) >= want) break; }
  MEMBAR();
}
__device__ __forceinline__ void until(u32 t0, u32 ticks) {
#pragma nounroll
  for (int it = 0; it < 4096 && (u32)__builtin_amdgcn_s_memrealtime() - t0 < ticks; ++it) __builtin_amdgcn_s_sleep(2);
}
__device__ __forceinline__ void lds_arrive(LAS u32* p, int lane) { MEMBAR(); if (lane == 0) __hip_atomic_fetch_add(p, 1u, __ATOMIC_RELAXED, __HIP_MEMORY_SCOPE_WORKGROUP); MEMBAR(); }
__device__ __forceinline__ void lds_set(LAS u32* p, int lane) { MEMBAR(); if (lane == 0) *(volatile LAS u32*)p = 1u; MEMBAR(); }
__device__ __forceinline__ s16x4 trd(LAS unsigned char* p) { return __builtin_amdgcn_ds_read_tr16_b64_v4i16((LAS s16x4*)p); }
__device__ __forceinline__ f32x4 mfma32v(v4u a, v4u b, f32x4 c) { return __builtin_amdgcn_mfma_f32_16x16x32_bf16(__builtin_bit_cast(bf16x8, a), __builtin_bit_cast(bf16x8, b), c, 0, 0, 0); }
__device__ __forceinline__ f32x4 mfma32(v4u a, s16x4 b0, s16x4 b1, f32x4 c) {
  s16x8 b = __builtin_shufflevector(b0, b1, 0, 1, 2, 3, 4, 5, 6, 7);
  return __builtin_amdgcn_mfma_f32_16x16x32_bf16(__builtin_bit_cast(bf16x8, a), __builtin_bit_cast(bf16x8, b), c, 0, 0, 0);
}
__device__ __forceinline__ f32x4 mfma16(s16x4 a, s16x4 b, f32x4 c) { return __builtin_amdgcn_mfma_f32_16x16x16bf16_1k(a, b, c, 0, 0, 0); }
__device__ __forceinline__ void glds_nt(const void* gsrc, u32 lds_dst) {
  u32 keep;
  asm volatile("s_mov_b32 %0, m0\n\ts_mov_b32 m0, %2\n\ts_nop 0\n\tglobal_load_lds_dwordx4 %1, off nt\n\ts_mov_b32 m0, %0" : "=&s"(keep) : "v"(gsrc), "s"(lds_dst) : "memory");
}
__device__ __forceinline__ void glds_nt_s(const void* sbase, u32 voff, u32 lds_dst) {
  u32 keep;
  asm volatile("s_mov_b32 %0, m0\n\ts_mov_b32 m0, %3\n\ts_nop 0\n\tglobal_load_lds_dwordx4 %1, %2 nt\n\ts_mov_b32 m0, %0" : "=&s"(keep) : "v"(voff), "s"(sbase), "s"(lds_dst) : "memory");
}
__device__ __forceinline__ void glds(const void* gsrc, u32 lds_dst) {
  u32 keep;
  asm volatile("s_mov_b32 %0, m0\n\ts_mov_b32 m0, %2\n\ts_nop 0\n\tglobal_load_lds_dwordx4 %1, off\n\ts_mov_b32 m0, %0" : "=&s"(keep) : "v"(gsrc), "s"(lds_dst) : "memory");
}
__device__ __forceinline__ v4u ldg(__amdgpu_buffer_rsrc_t rs, int off) { return __builtin_amdgcn_raw_buffer_load_b128(rs, off, 0, 16); }
__device__ __forceinline__ void stg(v4u v, __amdgpu_buffer_rsrc_t rs, int off) { __builtin_amdgcn_raw_buffer_store_b128(v, rs, off, 0, 16); }
template <int N, int SLP> __device__ __forceinline__ void gather(__amdgpu_buffer_rsrc_t rs, const int (&off)[N], v4u (&v)[N], u32 tl, u32 th) {
  u32 need = (1u << N) - 1u;
#pragma unroll
  for (int n = 0; n < N; ++n) v[n] = v4u{0, 0, 0, 0};
#pragma nounroll
  for (u32 it = 0; it < CAP_GLB; ++it) {
    v4u t[N];
#pragma unroll
    for (int n = 0; n < N; ++n) t[n] = ldg(rs, ((need >> n) & 1u) ? off[n] : OOB);
#pragma unroll
    for (int n = 0; n < N; ++n) if (((need >> n) & 1u) && g_bad(t[n], tl, th) == 0u) { v[n] = t[n]; need &= ~(1u << n); }
    if (__all(need == 0u)) break;
    MEMBAR();
    if (SLP) __builtin_amdgcn_s_sleep(SLP);
  }
}

__attribute__((amdgpu_waves_per_eu(3, 3)))
__global__ void __launch_bounds__(NTHR, 3) fused_decode(const u16* x, const u16* kc, const u16* vc, const u16* wq, const u16* wo, const u16* w1, const u16* b1,
    const u16* w2, const u16* b2, const u16* gw, u16* out, unsigned char* ws) {
  extern __shared__ __attribute__((aligned(16))) unsigned char lds_raw[];
  LAS unsigned char* L = (LAS unsigned char*)lds_raw;
  const u32 LB = (u32)(uintptr_t)L;
  LAS u32* SY = (LAS u32*)(L + L_SY);
  asm volatile("" :: "s"(x), "s"(kc), "s"(vc), "s"(wq), "s"(wo), "s"(w1), "s"(b1), "s"(w2), "s"(b2), "s"(gw), "s"(out), "s"(ws));
  const u32 tstart = (u32)__builtin_amdgcn_s_memrealtime();
  const int w = blockIdx.x, lane = threadIdx.x & 63, wave = __builtin_amdgcn_readfirstlane((int)(threadIdx.x >> 6));
  const int bh = w >> 3, s = w & 7, b = bh >> 4, kvh = bh & 15, m = b * 8 + s;
  const int phi = (w & 7) * 32 + (w >> 3);
  const int cbk = phi & 63, rb = phi >> 6;
  const int cb1 = w % 72, rb1 = w / 72;
  const int rho = phi >> 2, cb = phi & 3;
  const int g = lane >> 4, c = lane & 15;
  const __amdgpu_buffer_rsrc_t rs = __builtin_amdgcn_make_buffer_rsrc(ws, 0, WS_USED, 0x00020000);
  if (wave < 9) {
    const int r0 = 16 * wave + (lane >> 3);
    const u16* src = wq + (size_t)(144 * rb + r0) * NQ + 64 * cbk;
    const int sw0 = (r0 & 2) | ((r0 >> 1) & 4), sw1 = ((r0 + 8) & 2) | (((r0 + 8) >> 1) & 4);
    glds_nt(src + ((lane & 7) ^ sw0) * 8, LB + L_WT + wave * 2048);
    glds_nt(src + 8 * NQ + ((lane & 7) ^ sw1) * 8, LB + L_WT + wave * 2048 + 1024);
  }

  if (wave == 0) {
    __builtin_amdgcn_s_setprio(3);
    if (lane < 16) ((volatile LAS u32*)SY)[lane] = 0u;
    u64 E;
    asm volatile("global_load_dwordx2 %0, %1, off sc1" : "=v"(E) : "v"(ws) : "memory");
    glds(x + lane * 8, LB + L_XS);
    glds(x + 512 + lane * 8, LB + L_XS + 1024);
    if (lane < 16) glds(x + 1024 + lane * 8, LB + L_XS + 2048);
    {
      const u16* vw = vc + ((size_t)bh * SK + (SK - WIN) + s * 512) * 128;
      u32 keep;
      asm volatile("s_mov_b32 %0, m0\n\ts_mov_b32 m0, %2\n\ts_nop 0\n\tglobal_load_lds_dword %1, off nt\n\ts_mov_b32 m0, %0" : "=&s"(keep) : "v"(vw), "s"(LB + L_DUM) : "memory");
    }
    WAITL();
    __builtin_amdgcn_s_barrier();
    asm volatile("s_waitcnt vmcnt(1)" : "+v"(E) :: "memory");
    const u64 T = (E + 1ull) & 0xFFFFFFFFFFull;
    const u32 tl = (u32)__builtin_amdgcn_readfirstlane((int)((u32)(T & 0xFFu) << 24)), th = (u32)__builtin_amdgcn_readfirstlane((int)(u32)(T >> 8));
    if (lane == 0) { *(LAS u32*)(L + L_G + 8) = tl; *(LAS u32*)(L + L_G + 12) = th; }
    lds_set(SY + SY_T, lane);
    spin_lds_tight(SY + SY_W, 8);
    {
      f32x4 d[4];
      LAS unsigned char* xa = L + L_XS + (lane & 1) * (D * 2) + rb * 288;
      const int rq = 8 * g + (c >> 2);
#pragma unroll
      for (int t = 0; t < 4; ++t) d[t] = f32x4{0, 0, 0, 0};
#pragma unroll
      for (int ks = 0; ks < 4; ++ks) {
        const v4u a = *(LAS v4u*)(xa + ks * 64 + g * 16);
        const int r0 = 32 * ks + rq, r1 = r0 + 4;
        const int s0 = (r0 & 2) | ((r0 >> 1) & 4), s1 = (r1 & 2) | ((r1 >> 1) & 4);
#pragma unroll
        for (int t = 0; t < 4; ++t) {
          const int ch = 2 * t + ((c & 3) >> 1);
          d[t] = mfma32(a, trd(L + L_WT + r0 * 128 + (ch ^ s0) * 16 + (c & 1) * 8), trd(L + L_WT + r1 * 128 + (ch ^ s1) * 16 + (c & 1) * 8), d[t]);
        }
      }
      {
        const s16x4 a = *(LAS s16x4*)(xa + 256 + g * 8);
        const int r0 = 128 + 4 * g + (c >> 2), s0 = (r0 & 2) | ((r0 >> 1) & 4);
#pragma unroll
        for (int t = 0; t < 4; ++t) d[t] = mfma16(a, trd(L + L_WT + r0 * 128 + ((2 * t + ((c & 3) >> 1)) ^ s0) * 16 + (c & 1) * 8), d[t]);
      }
      const float v0 = g == 0 ? d[0][0] : g == 1 ? d[1][0] : g == 2 ? d[2][0] : d[3][0], v1 = g == 0 ? d[0][1] : g == 1 ? d[1][1] : g == 2 ? d[2][1] : d[3][1];
      const float rv = dpp<XOR1>((c & 1) ? v0 : v1);
      stg((c & 1) ? g_pack(rv, v1, tl, th) : g_pack(v0, rv, tl, th), rs, QP_OFF + (((c & 1) * 4 + rb) * NQ + 64 * cbk + 16 * g + (c & ~1)) * 8);
    }
    WAITL();
    lds_set(SY + SY_QP, lane);
    until(tstart, T_Q + (w * 37) % T_STAGGER);
    {
      int off[8]; v4u q[8];
#pragma unroll
      for (int n = 0; n < 8; ++n) off[n] = QP_OFF + (b * 4 * NQ + kvh * 256 + 2 * lane) * 8 + (n >> 1) * NQ * 8 + (n & 1) * 1024;
      gather<8, 4>(rs, off, q, tl, th);
#pragma unroll
      for (int h = 0; h < 2; ++h) {
        const float lo = (g_val(q[h].x) + g_val(q[2 + h].x)) + (g_val(q[4 + h].x) + g_val(q[6 + h].x)), hi = (g_val(q[h].z) + g_val(q[2 + h].z)) + (g_val(q[4 + h].z) + g_val(q[6 + h].z));
        *(LAS u32*)(L + L_QS + h * 256 + lane * 4) = pk_bf16(lo, hi);
      }
    }
    lds_set(SY + SY_Q, lane);
    spin_lds(SY + SY_C0, 8);
    {
      const int jj = lane & 7, hh = (lane >> 3) & 1;
      const float mw = *(LAS float*)(L + L_WST + jj * 16 + hh * 8), lw = *(LAS float*)(L + L_WST + jj * 16 + hh * 8 + 4);
      float mm = fmaxf(mw, dpp<XOR1>(mw)); mm = fmaxf(mm, dpp<XOR2>(mm)); mm = fmaxf(mm, dpp<HMIR>(mm));
      mm = g_val(g_lo(mm, 0u));
      const float gf = __builtin_amdgcn_exp2f(mw - mm);
      float ll = gf * lw; ll += dpp<XOR1>(ll); ll += dpp<XOR2>(ll); ll += dpp<HMIR>(ll);
      if (lane < 16) *(LAS float*)(L + L_GF + jj * 8 + hh * 4) = gf;
      lds_set(SY + SY_G, lane);
      if (lane < 16 && jj == 0) stg(g_pack(mm, ll, tl, th), rs, ST_OFF + ((kvh * 16 + m) * 4 + hh * 2) * 8);
    }
    float b2v = 0.f;
    if (w < 144) b2v = asf((u32)b2[4 * w + (lane & 3)] << 16);
#define H2_PUBLISH() do { \
      f32x2 ra[8], rb_[8]; \
      _Pragma("unroll") for (int j = 0; j < 8; ++j) { ra[j] = *(LAS f32x2*)(L + L_RED + j * 1024 + lane * 8); rb_[j] = *(LAS f32x2*)(L + L_RED + j * 1024 + 512 + lane * 8); } \
      SB(); \
      const f32x2 oa = ((ra[0] + ra[1]) + (ra[2] + ra[3])) + ((ra[4] + ra[5]) + (ra[6] + ra[7])), ob = ((rb_[0] + rb_[1]) + (rb_[2] + rb_[3])) + ((rb_[4] + rb_[5]) + (rb_[6] + rb_[7])); \
      stg(g_pack(oa[0], oa[1], tl, th), rs, h2pub); stg(g_pack(ob[0], ob[1], tl, th), rs, h2pub + 1024); } while (0)
    int h2pub = H2_OFF + ((kvh * 16 + m) * 256 + lane * 2) * 8;
    asm volatile("" : "+v"(h2pub));
    spin_lds_tight(SY + SY_C1, 8);
    H2_PUBLISH();
    if (w < 144) {
      spin_lds(SY + SY_H, 1);
      const float bias = (1.f - *(LAS float*)(L + L_G + ((lane >> 2) & 1) * 4)) * b2v;
      const int off = H3_OFF + (w * 256 + lane) * 16;
      v4u a0 = ldg(rs, off), a1 = ldg(rs, off + 1024), a2 = ldg(rs, off + 2048), a3 = ldg(rs, off + 3072);
#pragma nounroll
      for (u32 it = 0; it < CAP_GLB && !__all((g_bad(a0, tl, th) | g_bad(a1, tl, th) | g_bad(a2, tl, th) | g_bad(a3, tl, th)) == 0u); ++it) {
        a0 = ldg(rs, off); a1 = ldg(rs, off + 1024); a2 = ldg(rs, off + 2048); a3 = ldg(rs, off + 3072);
        MEMBAR();
      }
      float r0 = (g_val(a0.x) + g_val(a1.x)) + (g_val(a2.x) + g_val(a3.x)), r1 = (g_val(a0.z) + g_val(a1.z)) + (g_val(a2.z) + g_val(a3.z));
      const float t0 = r0 + dpp<ROR4>(r0), t1 = r1 + dpp<ROR4>(r1);
      const float u0 = t0 + dpp<ROR8>(t0), u1 = t1 + dpp<ROR8>(t1);
      float r = (lane & 4) ? u1 : u0;
      r = sum16_32(r);
      if (lane < 8) out[(lane >> 2) * D + 4 * w + (lane & 3)] = (u16)bf16r(r + bias);
      if (w == 0 && lane == 0) __hip_atomic_store((GAS u64*)ws, E + 1ull, __ATOMIC_RELAXED, __HIP_MEMORY_SCOPE_AGENT);
    }
  } else if (wave == 9) {
    __builtin_amdgcn_s_setprio(2);
    __builtin_amdgcn_s_barrier();
    u32 gwv[5];
#pragma unroll
    for (int n = 0; n < 5; ++n) gwv[n] = ((const u32*)gw)[min(n * 64 + lane, 287)];
    const u32 b1a = b1[72 * rho + lane], b1b = b1[72 * rho + 64 + (lane & 7)];
    spin_lds(SY + SY_T, 1);
    const u32 tl = (u32)__builtin_amdgcn_readfirstlane((int)*(LAS u32*)(L + L_G + 8)), th = (u32)__builtin_amdgcn_readfirstlane((int)*(LAS u32*)(L + L_G + 12));
    {
      float s0 = 0.f, s1 = 0.f;
#pragma unroll
      for (int n = 0; n < 5; ++n) { const int i = n * 64 + lane; if (i < 288) { s0 = dot2(*(LAS u32*)(L + L_XS + i * 4), gwv[n], s0); s1 = dot2(*(LAS u32*)(L + L_XS + D * 2 + i * 4), gwv[n], s1); } }
      s0 = wave_sum(s0); s1 = wave_sum(s1);
      const float g0 = 1.f / (1.f + __expf(-s0)), g1 = 1.f / (1.f + __expf(-s1));
      if (lane == 0) { *(LAS float*)(L + L_G) = g0; *(LAS float*)(L + L_G + 4) = g1; }
    }
    if (w < 216) {
      spin_lds(SY + SY_W1, 8);
      f32x4 d[4];
      LAS unsigned char* xa = L + L_XS + (lane & 1) * (D * 2) + rb1 * 384;
      const int rq = 8 * g + (c >> 2);
#pragma unroll
      for (int t = 0; t < 4; ++t) d[t] = f32x4{0, 0, 0, 0};
#pragma unroll
      for (int ks = 0; ks < 6; ++ks) {
        const v4u a = *(LAS v4u*)(xa + ks * 64 + g * 16);
        const int r0 = 32 * ks + rq, r1 = r0 + 4;
        const int s0 = (r0 & 2) | ((r0 >> 1) & 4), s1 = (r1 & 2) | ((r1 >> 1) & 4);
#pragma unroll
        for (int t = 0; t < 4; ++t) {
          const int ch = 2 * t + ((c & 3) >> 1);
          d[t] = mfma32(a, trd(L + L_WT + r0 * 128 + (ch ^ s0) * 16 + (c & 1) * 8), trd(L + L_WT + r1 * 128 + (ch ^ s1) * 16 + (c & 1) * 8), d[t]);
        }
      }
      const float v0 = g == 0 ? d[0][0] : g == 1 ? d[1][0] : g == 2 ? d[2][0] : d[3][0], v1 = g == 0 ? d[0][1] : g == 1 ? d[1][1] : g == 2 ? d[2][1] : d[3][1];
      stg(g_pack(v0, v1, tl, th), rs, HP_OFF + (rb1 * F + 64 * cb1 + lane) * 16);
    }
    until(tstart, T_H + (w * 53) % T_STAGGER);
    {
      int off[6]; v4u h[6];
#pragma unroll
      for (int n = 0; n < 3; ++n) { off[n] = HP_OFF + (n * F + 72 * rho + lane) * 16; off[3 + n] = HP_OFF + (n * F + 72 * rho + 64 + (lane & 7)) * 16; }
      gather<6, 4>(rs, off, h, tl, th);
      const float pa0 = (g_val(h[0].x) + g_val(h[1].x)) + g_val(h[2].x), pa1 = (g_val(h[0].z) + g_val(h[1].z)) + g_val(h[2].z);
      const float pb0 = (g_val(h[3].x) + g_val(h[4].x)) + g_val(h[5].x), pb1 = (g_val(h[3].z) + g_val(h[4].z)) + g_val(h[5].z);
      auto act = [](float p, u32 bias) { float v = asf(bf16r(asf(bf16r(p) << 16) + asf(bias << 16)) << 16); v = fmaxf(v, 0.f); return (u16)bf16r(v * v); };
      *(LAS u16*)(L + L_HC + lane * 2) = act(pa0, b1a); *(LAS u16*)(L + L_HC + 160 + lane * 2) = act(pa1, b1a);
      if (lane < 8) { *(LAS u16*)(L + L_HC + (64 + lane) * 2) = 0; *(LAS u16*)(L + L_HC + 160 + (64 + lane) * 2) = 0;
                      *(LAS u16*)(L + L_HC + (72 + lane) * 2) = act(pb0, b1b); *(LAS u16*)(L + L_HC + 160 + (72 + lane) * 2) = act(pb1, b1b); }
      lds_set(SY + SY_H, lane);
    }
  } else {
    const int j = wave - 1;
    const size_t kvoff = ((size_t)bh * SK + (SK - WIN) + s * 512) * 128;
    const u16* kb = kc + kvoff;
    const u16* vb = vc + kvoff;
    const u32 lin = j * 1024 + lane * 16;
    const __amdgpu_buffer_rsrc_t rv = __builtin_amdgcn_make_buffer_rsrc((void*)vb, 0, 131072, 0x00020000);
    const int rot = bh + (s >> 2) * 8;
#define KLOAD(n) glds_nt_s(kb + (((n) + rot) & 15) * 4096, j * 1024 + g * 256 + ((c ^ (n)) * 16), LB + L_K + j * 16384 + (n) * 1024)
#define VLOAD(n) V[n] = __builtin_amdgcn_raw_buffer_load_b128(rv, lin, (((n) + rot) & 15) * 8192, 2)
    v4u V[16], Wt[5];
#define W1LOAD(p) do { const int r_ = 8 * (3 * j + (p)) + (lane >> 3); \
      glds_nt(w1 + (size_t)(192 * rb1 + r_) * F + 64 * cb1 + ((lane & 7) ^ ((r_ & 2) | ((r_ >> 1) & 4))) * 8, LB + L_WT + (3 * j + (p)) * 1024); } while (0)
    int slot_k = 0; u32 tnow = 0;
#define S_(X) do { WAITV(7); { const u32 due_ = T_SLOT0 + (u32)slot_k * SLOT_NS / 10u; ++slot_k; \
      if (tnow < due_) { _Pragma("nounroll") for (int it_ = 0; it_ < 4096; ++it_) { tnow = (u32)__builtin_amdgcn_s_memrealtime() - tstart; if (tnow >= due_) break; __builtin_amdgcn_s_sleep(1); } } } X; SB(); } while (0)
    KLOAD(0); KLOAD(1); KLOAD(2); KLOAD(3);
    SB();
    __builtin_amdgcn_s_barrier();
    WAITV(4);
    lds_arrive(SY + SY_W, lane);
    S_(KLOAD(4)); S_(KLOAD(5)); S_(KLOAD(6)); S_(KLOAD(7)); S_(KLOAD(8)); S_(KLOAD(9));
    if (w < 216) {
      spin_lds(SY + SY_QP, 1);
      S_(W1LOAD(0)); S_(W1LOAD(1)); S_(W1LOAD(2));
      S_(KLOAD(10)); S_(KLOAD(11)); S_(KLOAD(12)); S_(KLOAD(13));
    } else {
      slot_k += 3;
      S_(KLOAD(10)); S_(KLOAD(11)); S_(KLOAD(12)); S_(KLOAD(13));
    }
    S_(KLOAD(14)); S_(KLOAD(15));
    S_(VLOAD(0)); S_(VLOAD(1));
    WAITV(8);
    lds_arrive(SY + SY_W1, lane);
    S_(VLOAD(2)); S_(VLOAD(3));
    S_(VLOAD(4)); S_(VLOAD(5)); S_(VLOAD(6)); S_(VLOAD(7));
    WAITV(8);
    spin_lds(SY + SY_Q, 1);
    f32x4 d[4];
#pragma unroll
    for (int t = 0; t < 4; ++t) d[t] = f32x4{0, 0, 0, 0};
#define QK(u) do { const v4u a_ = *(LAS v4u*)(L + L_QS + (c & 1) * 256 + (u) * 64 + g * 16); \
      _Pragma("unroll") for (int t = 0; t < 4; ++t) d[t] = mfma32v(a_, *(LAS v4u*)(L + L_K + j * 16384 + c * 1024 + t * 256 + (((4 * (u) + g) ^ c) * 16)), d[t]); \
      asm volatile("" : "+v"(d[0]), "+v"(d[1]), "+v"(d[2]), "+v"(d[3])); SB(); } while (0)
    S_(VLOAD(8)); QK(0); QK(1);
    S_(VLOAD(9)); QK(2); QK(3);
    S_(VLOAD(10));
    const float s0 = (g == 0 ? d[0][0] : g == 1 ? d[1][0] : g == 2 ? d[2][0] : d[3][0]) * C2;
    const float s1 = (g == 0 ? d[0][1] : g == 1 ? d[1][1] : g == 2 ? d[2][1] : d[3][1]) * C2;
    float m0 = wave_max(s0), m1 = wave_max(s1);
    asm volatile("" : "+v"(m0), "+v"(m1)); SB();
    S_(VLOAD(11));
    float p0 = __builtin_amdgcn_exp2f(s0 - m0), p1 = __builtin_amdgcn_exp2f(s1 - m1);
    const float l0 = wave_sum(p0), l1 = wave_sum(p1);
    if (lane == 0) *(LAS f32x4*)(L + L_WST + j * 16) = f32x4{m0, l0, m1, l1};
    lds_arrive(SY + SY_C0, lane);
    asm volatile("" : "+v"(p0), "+v"(p1)); SB();
    f32x2 acc[2][4];
#pragma unroll
    for (int k = 0; k < 4; ++k) { acc[0][k] = f32x2{0.f, 0.f}; acc[1][k] = f32x2{0.f, 0.f}; }
    const u16* wop = wo + (size_t)(64 * rho) * D + 144 * cb;
    const u16* w2p = w2 + (size_t)(72 * rho) * D + 144 * cb - 64 * D;
#define WLOAD(t) do { const int u_ = min((j + 8 * (t)) * 64 + lane, 136 * 18 - 1), r_ = u_ / 18; \
      Wt[t] = __builtin_nontemporal_load((const GAS v4u*)((r_ < 64 ? wop : w2p) + r_ * D + (u_ - r_ * 18) * 8)); } while (0)
#define PV(n) do { const float a_ = dpp<0x150 + (n)>(p0), b_ = dpp<0x150 + (n)>(p1); \
      _Pragma("unroll") for (int k = 0; k < 4; ++k) { const f32x2 v_ = {asf(V[n][k] << 16), asf(V[n][k] & 0xFFFF0000u)}; \
        acc[0][k] = __builtin_elementwise_fma(v_, f32x2{a_, a_}, acc[0][k]); acc[1][k] = __builtin_elementwise_fma(v_, f32x2{b_, b_}, acc[1][k]); } \
      asm volatile("" : "+v"(acc[0][0]), "+v"(acc[0][1]), "+v"(acc[0][2]), "+v"(acc[0][3]), "+v"(acc[1][0]), "+v"(acc[1][1]), "+v"(acc[1][2]), "+v"(acc[1][3])); SB(); } while (0)
    S_(VLOAD(12)); PV(0); PV(1); PV(2); PV(3);
    S_(VLOAD(13)); PV(4); PV(5); PV(6); PV(7);
    S_(VLOAD(14)); PV(8); PV(9); PV(10);
    S_(VLOAD(15)); PV(11);
    S_(WLOAD(0)); PV(12);
    S_(WLOAD(1)); PV(13);
    S_(WLOAD(2)); PV(14);
    spin_lds(SY + SY_G, 1);
    const float gf = *(LAS float*)(L + L_GF + j * 8 + (g >> 1) * 4);
    SB();
    S_(WLOAD(3)); WLOAD(4); SB(); PV(15);
    {
      float y[8], z[4];
#pragma unroll
      for (int k = 0; k < 8; ++k) { auto t = __builtin_amdgcn_permlane32_swap(asu(acc[0][k >> 1][k & 1]), asu(acc[1][k >> 1][k & 1]), false, false); y[k] = asf(t[0]) + asf(t[1]); }
#pragma unroll
      for (int k = 0; k < 4; ++k) { auto t = __builtin_amdgcn_permlane16_swap(asu(y[k]), asu(y[k + 4]), false, false); z[k] = asf(t[0]) + asf(t[1]); }
      *(LAS f32x4*)(L + L_RED + j * 1024 + (g >> 1) * 512 + c * 32 + (g & 1) * 16) = f32x4{z[0] * gf, z[1] * gf, z[2] * gf, z[3] * gf};
    }
    lds_arrive(SY + SY_C1, lane);
    const int kvc = rho >> 2, qt = rho & 3;
    const u32 tl = (u32)__builtin_amdgcn_readfirstlane((int)*(LAS u32*)(L + L_G + 8)), th = (u32)__builtin_amdgcn_readfirstlane((int)*(LAS u32*)(L + L_G + 12));
    const int h2off = (int)(H2_OFF + ((kvc * 16 + (j & 1) * 8 + (lane >> 3)) * 256 + 64 * qt + 16 * (j >> 1) + 2 * (lane & 7)) * 8);
    const int stoff = (int)(ST_OFF + ((kvc * 16 + (j & 1) * 8 + (lane >> 3)) * 4 + (qt >> 1) * 2) * 8);
    v4u st = ldg(rs, stoff);
    SB();
#pragma unroll
    for (int t = 0; t < 5; ++t) {
      const int u = (j + 8 * t) * 64 + lane;
      if (u < 136 * 18) *(LAS v4u*)(L + L_WI + u * 16) = Wt[t];
    }
    lds_arrive(SY + SY_C2, lane);
    spin_lds(SY + SY_C2, 8);
    spin_lds(SY + SY_H, 1);
    s16x4 bo[2][4];
    f32x4 dm[2];
    {
      LAS unsigned char* wb = L + L_WI + (4 * g + (c >> 2)) * 288 + (c & 3) * 8;
      LAS unsigned char* ha = L + L_HC + (lane & 1) * 160 + g * 8;
#pragma unroll
      for (int t = 0; t < 2; ++t) {
        if (t == 0 || j == 0) {
          LAS unsigned char* wt = wb + (t == 0 ? j * 32 : 256);
#pragma unroll
          for (int k = 0; k < 4; ++k) bo[t][k] = trd(wt + k * 16 * 288);
          dm[t] = f32x4{0, 0, 0, 0};
#pragma unroll
          for (int k = 0; k < 4; ++k) dm[t] = mfma16(*(LAS s16x4*)(ha + k * 32), trd(wt + (64 + k * 16) * 288), dm[t]);
          dm[t] = mfma16(*(LAS s16x4*)(ha + 128), trd(wt + (64 + 56) * 288), dm[t]);
        }
      }
    }
    const float g0 = *(LAS float*)(L + L_G), g1 = *(LAS float*)(L + L_G + 4);
#pragma nounroll
    for (u32 it = 0; it < CAP_GLB && !__all(g_bad(st, tl, th) == 0u); ++it) { st = ldg(rs, stoff); MEMBAR(); }
    float fs;
    {
      const float mv = g_val(st.x);
      const float M = max16_32(fmaxf(mv, dpp<ROR8>(mv)));
      const float e = __builtin_amdgcn_exp2f(mv - M);
      float l = e * g_val(st.z); l = sum16_32(l + dpp<ROR8>(l));
      fs = e / l;
      asm volatile("" : "+v"(fs));
    }
    v4u o = ldg(rs, h2off);
#pragma nounroll
    for (u32 it = 0; it < CAP_GLB && !__all(g_bad(o, tl, th) == 0u); ++it) { o = ldg(rs, h2off); MEMBAR(); }
    {
      float a0 = fs * g_val(o.x), a1 = fs * g_val(o.z);
      a0 += dpp<ROR8>(a0); a1 += dpp<ROR8>(a1); a0 = sum16_32(a0); a1 = sum16_32(a1);
      if (lane < 8) *(LAS u32*)(L + L_OC + (j & 1) * 128 + (16 * (j >> 1) + 2 * lane) * 2) = pk_bf16(a0, a1);
    }
    lds_arrive(SY + SY_C3, lane);
    spin_lds_tight(SY + SY_C3, 8);
    {
      LAS unsigned char* oa = L + L_OC + (lane & 1) * 128 + g * 8;
      s16x4 ao[4];
#pragma unroll
      for (int k = 0; k < 4; ++k) ao[k] = *(LAS s16x4*)(oa + k * 32);
#pragma unroll
      for (int t = 0; t < 2; ++t) {
        if (t == 0 || j == 0) {
          f32x4 da = {0, 0, 0, 0};
#pragma unroll
          for (int k = 0; k < 4; ++k) da = mfma16(ao[k], bo[t][k], da);
          const float v0 = g0 * da[0] + (1.f - g0) * dm[t][0], v1 = g1 * da[1] + (1.f - g1) * dm[t][1];
          const int col = 144 * cb + (t == 0 ? 16 * j : 128) + c;
          if (lane < 16) stg(g_pack(v0, v1, tl, th), rs, H3_OFF + (((col >> 2) * 64 + rho) * 4 + (col & 3)) * 16);
        }
      }
    }
  }
}

extern "C" void kernel_launch(void* const* d_in, const int* in_sizes, int n_in, void* d_out, int out_size, void* d_ws, size_t ws_size, hipStream_t stream) {
  (void)in_sizes; (void)n_in; (void)out_size; (void)ws_size;
  (void)hipFuncSetAttribute((const void*)fused_decode, hipFuncAttributeMaxDynamicSharedMemorySize, L_END);
  fused_decode<<<NWG, NTHR, L_END, stream>>>((const u16*)d_in[0], (const u16*)d_in[1], (const u16*)d_in[2], (const u16*)d_in[3], (const u16*)d_in[4],
      (const u16*)d_in[6], (const u16*)d_in[7], (const u16*)d_in[8], (const u16*)d_in[9], (const u16*)d_in[10], (u16*)d_out, (unsigned char*)d_ws);
}
```

```cpp
#include <hip/hip_runtime.h>
#include <stdint.h>

typedef unsigned u32; typedef unsigned long long u64; typedef unsigned short u16;
typedef u32 v4u __attribute__((ext_vector_type(4)));
typedef float f32x4 __attribute__((ext_vector_type(4)));
typedef float f32x2 __attribute__((ext_vector_type(2)));
typedef short s16x4 __attribute__((ext_vector_type(4)));
typedef short s16x8 __attribute__((ext_vector_type(8)));
typedef __bf16 bf16x8 __attribute__((ext_vector_type(8)));
typedef __bf16 bf16x2 __attribute__((ext_vector_type(2)));
#define GAS __attribute__((address_space(1)))
#define LAS __attribute__((address_space(3)))

constexpr int D = 576, NQ = 4096, F = 4608, SK = 32768, WIN = 4096;
constexpr int NWG = 256, NTHR = 640;
constexpr u32 T_SLOT0 = 170, SLOT_NS = 295, T_Q = 540, T_H = 1000, T_STAGGER = 80;
constexpr u32 QP_OFF = 4096, HP_OFF = QP_OFF + 2 * 4 * NQ * 8, ST_OFF = HP_OFF + 3 * F * 16, H2_OFF = ST_OFF + 16 * 16 * 4 * 8,
              H3_OFF = H2_OFF + 16 * 16 * 256 * 8, WS_USED = H3_OFF + 144 * 64 * 4 * 16;
constexpr int L_K = 0, L_RED = 0, L_WI = 8192, L_WT = 131072  , L_XS = L_WT + 192 * 128, L_QS = L_XS + 2 * D * 2,
              L_WST = L_QS + 512, L_GF = L_WST + 128, L_HC = L_GF + 64, L_OC = L_HC + 2 * 80 * 2, L_G = L_OC + 2 * 64 * 2,
              L_SY = L_G + 32, L_DUM = L_SY + 64, L_END = L_DUM + 256;
static_assert(L_END <= 163840 && L_WI + 136 * 288 <= 131072 && (L_HC % 16) == 0 && (L_OC % 16) == 0 && (L_QS % 16) == 0, "LDS map");
enum { SY_Q = 0, SY_C0, SY_G, SY_H, SY_C1, SY_C2, SY_C3, SY_W, SY_QP, SY_W1, SY_T };
constexpr int OOB = 0x40000000;
constexpr u32 CAP_LDS = 1u << 20, CAP_GLB = 1u << 16;
constexpr float C2 = 0.08838834764831845f * 1.4426950408889634f;

__device__ __forceinline__ float asf(u32 x) { return __builtin_bit_cast(float, x); }
__device__ __forceinline__ u32 asu(float x) { return __builtin_bit_cast(u32, x); }
__device__ __forceinline__ u32 bf16r(float f) { u32 u = asu(f); return (u + 0x7FFFu + ((u >> 16) & 1u)) >> 16; }
__device__ __forceinline__ u32 pk_bf16(float lo, float hi) { return bf16r(lo) | (bf16r(hi) << 16); }
__device__ __forceinline__ float dot2(u32 a, u32 b, float c) { return __builtin_amdgcn_fdot2_f32_bf16(__builtin_bit_cast(bf16x2, a), __builtin_bit_cast(bf16x2, b), c, false); }
template <int CTRL> __device__ __forceinline__ float dpp(float x) { return asf((u32)__builtin_amdgcn_mov_dpp((int)asu(x), CTRL, 0xf, 0xf, true)); }
constexpr int XOR1 = 0xB1, XOR2 = 0x4E, HMIR = 0x141, ROR8 = 0x128, ROR4 = 0x124, ROR2 = 0x122, ROR1 = 0x121;
__device__ __forceinline__ float sum16_32(float x) {
  auto s = __builtin_amdgcn_permlane16_swap(asu(x), asu(x), false, false); x = asf(s[0]) + asf(s[1]);
  auto t = __builtin_amdgcn_permlane32_swap(asu(x), asu(x), false, false); return asf(t[0]) + asf(t[1]);
}
__device__ __forceinline__ float max16_32(float x) {
  auto s = __builtin_amdgcn_permlane16_swap(asu(x), asu(x), false, false); x = fmaxf(asf(s[0]), asf(s[1]));
  auto t = __builtin_amdgcn_permlane32_swap(asu(x), asu(x), false, false); return fmaxf(asf(t[0]), asf(t[1]));
}
__device__ __forceinline__ float wave_sum(float x) { x += dpp<ROR1>(x); x += dpp<ROR2>(x); x += dpp<ROR4>(x); x += dpp<ROR8>(x); return sum16_32(x); }
__device__ __forceinline__ float wave_max(float x) { x = fmaxf(x, dpp<ROR1>(x)); x = fmaxf(x, dpp<ROR2>(x)); x = fmaxf(x, dpp<ROR4>(x)); x = fmaxf(x, dpp<ROR8>(x)); return max16_32(x); }
__device__ __forceinline__ u32 g_lo(float v, u32 tl) { return ((asu(v) + 0x80u) >> 8) | tl; }
__device__ __forceinline__ float g_val(u32 lo) { return asf(lo << 8); }
__device__ __forceinline__ u32 g_bad(v4u q, u32 tl, u32 th) { return (((q.x ^ tl) | (q.z ^ tl)) & 0xFF000000u) | (q.y ^ th) | (q.w ^ th); }
__device__ __forceinline__ v4u g_pack(float a, float b, u32 tl, u32 th) { return v4u{g_lo(a, tl), th, g_lo(b, tl), th}; }
#define WAITV(n) asm volatile("s_waitcnt vmcnt(" #n ")" ::: "memory")
#define WAITL() asm volatile("s_waitcnt lgkmcnt(0)" ::: "memory")
#define MEMBAR() asm volatile("" ::: "memory")
#define SB() __builtin_amdgcn_sched_barrier(0)
__device__ __forceinline__ void spin_lds(LAS u32* p, u32 want) {
#pragma nounroll
  for (u32 n = 0; n < CAP_LDS; ++n) {
    if ((u32)__builtin_amdgcn_readfirstlane((int)*(volatile LAS u32*)p) >= want) break;
    __builtin_amdgcn_s_sleep(1);
  }
  MEMBAR();
}
__device__ __forceinline__ void spin_lds_tight(LAS u32* p, u32 want) {
#pragma nounroll
  for (u32 n = 0; n < CAP_LDS; ++n) { if ((u32)__builtin_amdgcn_readfirstlane((int)*(volatile LAS u32*)p) >= want) break; }
  MEMBAR();
}
__device__ __forceinline__ void until(u32 t0, u32 ticks) {
#pragma nounroll
  for (int it = 0; it < 4096 && (u32)__builtin_amdgcn_s_memrealtime() - t0 < ticks; ++it) __builtin_amdgcn_s_sleep(2);
}
__device__ __forceinline__ void lds_arrive(LAS u32* p, int lane) { MEMBAR(); if (lane == 0) __hip_atomic_fetch_add(p, 1u, __ATOMIC_RELAXED, __HIP_MEMORY_SCOPE_WORKGROUP); MEMBAR(); }
__device__ __forceinline__ void lds_set(LAS u32* p, int lane) { MEMBAR(); if (lane == 0) *(volatile LAS u32*)p = 1u; MEMBAR(); }
__device__ __forceinline__ s16x4 trd(LAS unsigned char* p) { return __builtin_amdgcn_ds_read_tr16_b64_v4i16((LAS s16x4*)p); }
__device__ __forceinline__ f32x4 mfma32v(v4u a, v4u b, f32x4 c) { return __builtin_amdgcn_mfma_f32_16x16x32_bf16(__builtin_bit_cast(bf16x8, a), __builtin_bit_cast(bf16x8, b), c, 0, 0, 0); }
__device__ __forceinline__ f32x4 mfma32(v4u a, s16x4 b0, s16x4 b1, f32x4 c) {
  s16x8 b = __builtin_shufflevector(b0, b1, 0, 1, 2, 3, 4, 5, 6, 7);
  return __builtin_amdgcn_mfma_f32_16x16x32_bf16(__builtin_bit_cast(bf16x8, a), __builtin_bit_cast(bf16x8, b), c, 0, 0, 0);
}
__device__ __forceinline__ f32x4 mfma16(s16x4 a, s16x4 b, f32x4 c) { return __builtin_amdgcn_mfma_f32_16x16x16bf16_1k(a, b, c, 0, 0, 0); }
__device__ __forceinline__ void glds_nt(const void* gsrc, u32 lds_dst) {
  u32 keep;
  asm volatile("s_mov_b32 %0, m0\n\ts_mov_b32 m0, %2\n\ts_nop 0\n\tglobal_load_lds_dwordx4 %1, off nt\n\ts_mov_b32 m0, %0" : "=&s"(keep) : "v"(gsrc), "s"(lds_dst) : "memory");
}
__device__ __forceinline__ void glds_nt_s(const void* sbase, u32 voff, u32 lds_dst) {
  u32 keep;
  asm volatile("s_mov_b32 %0, m0\n\ts_mov_b32 m0, %3\n\ts_nop 0\n\tglobal_load_lds_dwordx4 %1, %2 nt\n\ts_mov_b32 m0, %0" : "=&s"(keep) : "v"(voff), "s"(sbase), "s"(lds_dst) : "memory");
}
__device__ __forceinline__ void glds(const void* gsrc, u32 lds_dst) {
  u32 keep;
  asm volatile("s_mov_b32 %0, m0\n\ts_mov_b32 m0, %2\n\ts_nop 0\n\tglobal_load_lds_dwordx4 %1, off\n\ts_mov_b32 m0, %0" : "=&s"(keep) : "v"(gsrc), "s"(lds_dst) : "memory");
}
__device__ __forceinline__ v4u ldg(__amdgpu_buffer_rsrc_t rs, int off) { return __builtin_amdgcn_raw_buffer_load_b128(rs, off, 0, 16); }
__device__ __forceinline__ void stg(v4u v, __amdgpu_buffer_rsrc_t rs, int off) { __builtin_amdgcn_raw_buffer_store_b128(v, rs, off, 0, 16); }
template <int N, int SLP> __device__ __forceinline__ void gather(__amdgpu_buffer_rsrc_t rs, const int (&off)[N], v4u (&v)[N], u32 tl, u32 th) {
  u32 need = (1u << N) - 1u;
#pragma unroll
  for (int n = 0; n < N; ++n) v[n] = v4u{0, 0, 0, 0};
#pragma nounroll
  for (u32 it = 0; it < CAP_GLB; ++it) {
    v4u t[N];
#pragma unroll
    for (int n = 0; n < N; ++n) t[n] = ldg(rs, ((need >> n) & 1u) ? off[n] : OOB);
#pragma unroll
    for (int n = 0; n < N; ++n) if (((need >> n) & 1u) && g_bad(t[n], tl, th) == 0u) { v[n] = t[n]; need &= ~(1u << n); }
    if (__all(need == 0u)) break;
    MEMBAR();
    if (SLP) __builtin_amdgcn_s_sleep(SLP);
  }
}

__attribute__((amdgpu_waves_per_eu(3, 3)))
__global__ void __launch_bounds__(NTHR, 3) fused_decode(const u16* x, const u16* kc, const u16* vc, const u16* wq, const u16* wo, const u16* w1, const u16* b1,
    const u16* w2, const u16* b2, const u16* gw, u16* out, unsigned char* ws) {
  extern __shared__ __attribute__((aligned(16))) unsigned char lds_raw[];
  LAS unsigned char* L = (LAS unsigned char*)lds_raw;
  const u32 LB = (u32)(uintptr_t)L;
  LAS u32* SY = (LAS u32*)(L + L_SY);
  asm volatile("" :: "s"(x), "s"(kc), "s"(vc), "s"(wq), "s"(wo), "s"(w1), "s"(b1), "s"(w2), "s"(b2), "s"(gw), "s"(out), "s"(ws));
  const u32 tstart = (u32)__builtin_amdgcn_s_memrealtime();
  const int w = blockIdx.x, lane = threadIdx.x & 63, wave = __builtin_amdgcn_readfirstlane((int)(threadIdx.x >> 6));
  const int bh = w >> 3, s = w & 7, b = bh >> 4, kvh = bh & 15, m = b * 8 + s;
  const int phi = (w & 7) * 32 + (w >> 3);
  const int cbk = phi & 63, rb = phi >> 6;
  const int cb1 = w % 72, rb1 = w / 72;
  const int rho = phi >> 2, cb = phi & 3;
  const int g = lane >> 4, c = lane & 15;
  const __amdgpu_buffer_rsrc_t rs = __builtin_amdgcn_make_buffer_rsrc(ws, 0, WS_USED, 0x00020000);
  if (wave < 9) {
    const int r0 = 16 * wave + (lane >> 3);
    const u16* src = wq + (size_t)(144 * rb + r0) * NQ + 64 * cbk;
    const int sw0 = (r0 & 2) | ((r0 >> 1) & 4), sw1 = ((r0 + 8) & 2) | (((r0 + 8) >> 1) & 4);
    glds_nt(src + ((lane & 7) ^ sw0) * 8, LB + L_WT + wave * 2048);
    glds_nt(src + 8 * NQ + ((lane & 7) ^ sw1) * 8, LB + L_WT + wave * 2048 + 1024);
  }

  if (wave == 0) {
    __builtin_amdgcn_s_setprio(3);
    if (lane < 16) ((volatile LAS u32*)SY)[lane] = 0u;
    u64 E;
    asm volatile("global_load_dwordx2 %0, %1, off sc1" : "=v"(E) : "v"(ws) : "memory");
    glds(x + lane * 8, LB + L_XS);
    glds(x + 512 + lane * 8, LB + L_XS + 1024);
    if (lane < 16) glds(x + 1024 + lane * 8, LB + L_XS + 2048);
    {
      const u16* vw = vc + ((size_t)bh * SK + (SK - WIN) + s * 512) * 128;
      u32 keep;
      asm volatile("s_mov_b32 %0, m0\n\ts_mov_b32 m0, %2\n\ts_nop 0\n\tglobal_load_lds_dword %1, off nt\n\ts_mov_b32 m0, %0" : "=&s"(keep) : "v"(vw), "s"(LB + L_DUM) : "memory");
    }
    WAITL();
    __builtin_amdgcn_s_barrier();
    asm volatile("s_waitcnt vmcnt(1)" : "+v"(E) :: "memory");
    const u64 T = (E + 1ull) & 0xFFFFFFFFFFull;
    const u32 tl = (u32)__builtin_amdgcn_readfirstlane((int)((u32)(T & 0xFFu) << 24)), th = (u32)__builtin_amdgcn_readfirstlane((int)(u32)(T >> 8));
    if (lane == 0) { *(LAS u32*)(L + L_G + 8) = tl; *(LAS u32*)(L + L_G + 12) = th; }
    lds_set(SY + SY_T, lane);
    spin_lds_tight(SY + SY_W, 8);
    {
      f32x4 d[4];
      LAS unsigned char* xa = L + L_XS + (lane & 1) * (D * 2) + rb * 288;
      const int rq = 8 * g + (c >> 2);
#pragma unroll
      for (int t = 0; t < 4; ++t) d[t] = f32x4{0, 0, 0, 0};
#pragma unroll
      for (int ks = 0; ks < 4; ++ks) {
        const v4u a = *(LAS v4u*)(xa + ks * 64 + g * 16);
        const int r0 = 32 * ks + rq, r1 = r0 + 4;
        const int s0 = (r0 & 2) | ((r0 >> 1) & 4), s1 = (r1 & 2) | ((r1 >> 1) & 4);
#pragma unroll
        for (int t = 0; t < 4; ++t) {
          const int ch = 2 * t + ((c & 3) >> 1);
          d[t] = mfma32(a, trd(L + L_WT + r0 * 128 + (ch ^ s0) * 16 + (c & 1) * 8), trd(L + L_WT + r1 * 128 + (ch ^ s1) * 16 + (c & 1) * 8), d[t]);
        }
      }
      {
        const s16x4 a = *(LAS s16x4*)(xa + 256 + g * 8);
        const int r0 = 128 + 4 * g + (c >> 2), s0 = (r0 & 2) | ((r0 >> 1) & 4);
#pragma unroll
        for (int t = 0; t < 4; ++t) d[t] = mfma16(a, trd(L + L_WT + r0 * 128 + ((2 * t + ((c & 3) >> 1)) ^ s0) * 16 + (c & 1) * 8), d[t]);
      }
      const float v0 = g == 0 ? d[0][0] : g == 1 ? d[1][0] : g == 2 ? d[2][0] : d[3][0], v1 = g == 0 ? d[0][1] : g == 1 ? d[1][1] : g == 2 ? d[2][1] : d[3][1];
      const float rv = dpp<XOR1>((c & 1) ? v0 : v1);
      stg((c & 1) ? g_pack(rv, v1, tl, th) : g_pack(v0, rv, tl, th), rs, QP_OFF + (((c & 1) * 4 + rb) * NQ + 64 * cbk + 16 * g + (c & ~1)) * 8);
    }
    WAITL();
    lds_set(SY + SY_QP, lane);
    until(tstart, T_Q + (w * 37) % T_STAGGER);
    {
      int off[8]; v4u q[8];
#pragma unroll
      for (int n = 0; n < 8; ++n) off[n] = QP_OFF + (b * 4 * NQ + kvh * 256 + 2 * lane) * 8 + (n >> 1) * NQ * 8 + (n & 1) * 1024;
      gather<8, 4>(rs, off, q, tl, th);
#pragma unroll
      for (int h = 0; h < 2; ++h) {
        const float lo = (g_val(q[h].x) + g_val(q[2 + h].x)) + (g_val(q[4 + h].x) + g_val(q[6 + h].x)), hi = (g_val(q[h].z) + g_val(q[2 + h].z)) + (g_val(q[4 + h].z) + g_val(q[6 + h].z));
        *(LAS u32*)(L + L_QS + h * 256 + lane * 4) = pk_bf16(lo, hi);
      }
    }
    lds_set(SY + SY_Q, lane);
    spin_lds(SY + SY_C0, 8);
    {
      const int jj = lane & 7, hh = (lane >> 3) & 1;
      const float mw = *(LAS float*)(L + L_WST + jj * 16 + hh * 8), lw = *(LAS float*)(L + L_WST + jj * 16 + hh * 8 + 4);
      float mm = fmaxf(mw, dpp<XOR1>(mw)); mm = fmaxf(mm, dpp<XOR2>(mm)); mm = fmaxf(mm, dpp<HMIR>(mm));
      mm = g_val(g_lo(mm, 0u));
      const float gf = __builtin_amdgcn_exp2f(mw - mm);
      float ll = gf * lw; ll += dpp<XOR1>(ll); ll += dpp<XOR2>(ll); ll += dpp<HMIR>(ll);
      if (lane < 16) *(LAS float*)(L + L_GF + jj * 8 + hh * 4) = gf;
      lds_set(SY + SY_G, lane);
      if (lane < 16 && jj == 0) stg(g_pack(mm, ll, tl, th), rs, ST_OFF + ((kvh * 16 + m) * 4 + hh * 2) * 8);
    }
    float b2v = 0.f;
    if (w < 144) b2v = asf((u32)b2[4 * w + (lane & 3)] << 16);
#define H2_PUBLISH() do { \
      f32x2 ra[8], rb_[8]; \
      _Pragma("unroll") for (int j = 0; j < 8; ++j) { ra[j] = *(LAS f32x2*)(L + L_RED + j * 1024 + lane * 8); rb_[j] = *(LAS f32x2*)(L + L_RED + j * 1024 + 512 + lane * 8); } \
      SB(); \
      const f32x2 oa = ((ra[0] + ra[1]) + (ra[2] + ra[3])) + ((ra[4] + ra[5]) + (ra[6] + ra[7])), ob = ((rb_[0] + rb_[1]) + (rb_[2] + rb_[3])) + ((rb_[4] + rb_[5]) + (rb_[6] + rb_[7])); \
      stg(g_pack(oa[0], oa[1], tl, th), rs, h2pub); stg(g_pack(ob[0], ob[1], tl, th), rs, h2pub + 1024); } while (0)
    int h2pub = H2_OFF + ((kvh * 16 + m) * 256 + lane * 2) * 8;
    asm volatile("" : "+v"(h2pub));
    spin_lds_tight(SY + SY_C1, 8);
    H2_PUBLISH();
    if (w < 144) {
      spin_lds(SY + SY_H, 1);
      const float bias = (1.f - *(LAS float*)(L + L_G + ((lane >> 2) & 1) * 4)) * b2v;
      const int off = H3_OFF + (w * 256 + lane) * 16;
      v4u a0 = ldg(rs, off), a1 = ldg(rs, off + 1024), a2 = ldg(rs, off + 2048), a3 = ldg(rs, off + 3072);
#pragma nounroll
      for (u32 it = 0; it < CAP_GLB && !__all((g_bad(a0, tl, th) | g_bad(a1, tl, th) | g_bad(a2, tl, th) | g_bad(a3, tl, th)) == 0u); ++it) {
        a0 = ldg(rs, off); a1 = ldg(rs, off + 1024); a2 = ldg(rs, off + 2048); a3 = ldg(rs, off + 3072);
        MEMBAR();
      }
      float r0 = (g_val(a0.x) + g_val(a1.x)) + (g_val(a2.x) + g_val(a3.x)), r1 = (g_val(a0.z) + g_val(a1.z)) + (g_val(a2.z) + g_val(a3.z));
      const float t0 = r0 + dpp<ROR4>(r0), t1 = r1 + dpp<ROR4>(r1);
      const float u0 = t0 + dpp<ROR8>(t0), u1 = t1 + dpp<ROR8>(t1);
      float r = (lane & 4) ? u1 : u0;
      r = sum16_32(r);
      const u32 hv = bf16r(r + bias);
      const u32 pr = hv | (asu(dpp<XOR1>(asf(hv))) << 16);
      const u32 hi = asu(dpp<XOR2>(asf(pr)));
      if ((lane & 3) == 0 && lane < 8) __hip_atomic_store((GAS u64*)(out + (lane >> 2) * D + 4 * w), ((u64)hi << 32) | pr, __ATOMIC_RELAXED, __HIP_MEMORY_SCOPE_AGENT);
      if (w == 0 && lane == 0) __hip_atomic_store((GAS u64*)ws, E + 1ull, __ATOMIC_RELAXED, __HIP_MEMORY_SCOPE_AGENT);
    }
  } else if (wave == 9) {
    __builtin_amdgcn_s_setprio(2);
    __builtin_amdgcn_s_barrier();
    u32 gwv[5];
#pragma unroll
    for (int n = 0; n < 5; ++n) gwv[n] = ((const u32*)gw)[min(n * 64 + lane, 287)];
    const u32 b1a = b1[72 * rho + lane], b1b = b1[72 * rho + 64 + (lane & 7)];
    spin_lds(SY + SY_T, 1);
    const u32 tl = (u32)__builtin_amdgcn_readfirstlane((int)*(LAS u32*)(L + L_G + 8)), th = (u32)__builtin_amdgcn_readfirstlane((int)*(LAS u32*)(L + L_G + 12));
    {
      float s0 = 0.f, s1 = 0.f;
#pragma unroll
      for (int n = 0; n < 5; ++n) { const int i = n * 64 + lane; if (i < 288) { s0 = dot2(*(LAS u32*)(L + L_XS + i * 4), gwv[n], s0); s1 = dot2(*(LAS u32*)(L + L_XS + D * 2 + i * 4), gwv[n], s1); } }
      s0 = wave_sum(s0); s1 = wave_sum(s1);
      const float g0 = 1.f / (1.f + __expf(-s0)), g1 = 1.f / (1.f + __expf(-s1));
      if (lane == 0) { *(LAS float*)(L + L_G) = g0; *(LAS float*)(L + L_G + 4) = g1; }
    }
    if (w < 216) {
      spin_lds(SY + SY_W1, 8);
      f32x4 d[4];
      LAS unsigned char* xa = L + L_XS + (lane & 1) * (D * 2) + rb1 * 384;
      const int rq = 8 * g + (c >> 2);
#pragma unroll
      for (int t = 0; t < 4; ++t) d[t] = f32x4{0, 0, 0, 0};
#pragma unroll
      for (int ks = 0; ks < 6; ++ks) {
        const v4u a = *(LAS v4u*)(xa + ks * 64 + g * 16);
        const int r0 = 32 * ks + rq, r1 = r0 + 4;
        const int s0 = (r0 & 2) | ((r0 >> 1) & 4), s1 = (r1 & 2) | ((r1 >> 1) & 4);
#pragma unroll
        for (int t = 0; t < 4; ++t) {
          const int ch = 2 * t + ((c & 3) >> 1);
          d[t] = mfma32(a, trd(L + L_WT + r0 * 128 + (ch ^ s0) * 16 + (c & 1) * 8), trd(L + L_WT + r1 * 128 + (ch ^ s1) * 16 + (c & 1) * 8), d[t]);
        }
      }
      const float v0 = g == 0 ? d[0][0] : g == 1 ? d[1][0] : g == 2 ? d[2][0] : d[3][0], v1 = g == 0 ? d[0][1] : g == 1 ? d[1][1] : g == 2 ? d[2][1] : d[3][1];
      stg(g_pack(v0, v1, tl, th), rs, HP_OFF + (rb1 * F + 64 * cb1 + lane) * 16);
    }
    until(tstart, T_H + (w * 53) % T_STAGGER);
    {
      int off[6]; v4u h[6];
#pragma unroll
      for (int n = 0; n < 3; ++n) { off[n] = HP_OFF + (n * F + 72 * rho + lane) * 16; off[3 + n] = HP_OFF + (n * F + 72 * rho + 64 + (lane & 7)) * 16; }
      gather<6, 4>(rs, off, h, tl, th);
      const float pa0 = (g_val(h[0].x) + g_val(h[1].x)) + g_val(h[2].x), pa1 = (g_val(h[0].z) + g_val(h[1].z)) + g_val(h[2].z);
      const float pb0 = (g_val(h[3].x) + g_val(h[4].x)) + g_val(h[5].x), pb1 = (g_val(h[3].z) + g_val(h[4].z)) + g_val(h[5].z);
      auto act = [](float p, u32 bias) { float v = asf(bf16r(asf(bf16r(p) << 16) + asf(bias << 16)) << 16); v = fmaxf(v, 0.f); return (u16)bf16r(v * v); };
      *(LAS u16*)(L + L_HC + lane * 2) = act(pa0, b1a); *(LAS u16*)(L + L_HC + 160 + lane * 2) = act(pa1, b1a);
      if (lane < 8) { *(LAS u16*)(L + L_HC + (64 + lane) * 2) = 0; *(LAS u16*)(L + L_HC + 160 + (64 + lane) * 2) = 0;
                      *(LAS u16*)(L + L_HC + (72 + lane) * 2) = act(pb0, b1b); *(LAS u16*)(L + L_HC + 160 + (72 + lane) * 2) = act(pb1, b1b); }
      lds_set(SY + SY_H, lane);
    }
  } else {
    const int j = wave - 1;
    const size_t kvoff = ((size_t)bh * SK + (SK - WIN) + s * 512) * 128;
    const u16* kb = kc + kvoff;
    const u16* vb = vc + kvoff;
    const u32 lin = j * 1024 + lane * 16;
    const __amdgpu_buffer_rsrc_t rv = __builtin_amdgcn_make_buffer_rsrc((void*)vb, 0, 131072, 0x00020000);
    const int rot = bh + (s >> 2) * 8;
#define KLOAD(n) glds_nt_s(kb + (((n) + rot) & 15) * 4096, j * 1024 + g * 256 + ((c ^ (n)) * 16), LB + L_K + j * 16384 + (n) * 1024)
#define VLOAD(n) V[n] = __builtin_amdgcn_raw_buffer_load_b128(rv, lin, (((n) + rot) & 15) * 8192, 2)
    v4u V[16], Wt[5];
#define W1LOAD(p) do { const int r_ = 8 * (3 * j + (p)) + (lane >> 3); \
      glds_nt(w1 + (size_t)(192 * rb1 + r_) * F + 64 * cb1 + ((lane & 7) ^ ((r_ & 2) | ((r_ >> 1) & 4))) * 8, LB + L_WT + (3 * j + (p)) * 1024); } while (0)
    int slot_k = 0; u32 tnow = 0;
#define S_(X) do { WAITV(7); { const u32 due_ = T_SLOT0 + (u32)slot_k * SLOT_NS / 10u; ++slot_k; \
      if (tnow < due_) { _Pragma("nounroll") for (int it_ = 0; it_ < 4096; ++it_) { tnow = (u32)__builtin_amdgcn_s_memrealtime() - tstart; if (tnow >= due_) break; __builtin_amdgcn_s_sleep(1); } } } X; SB(); } while (0)
    KLOAD(0); KLOAD(1); KLOAD(2); KLOAD(3);
    SB();
    __builtin_amdgcn_s_barrier();
    WAITV(4);
    lds_arrive(SY + SY_W, lane);
    S_(KLOAD(4)); S_(KLOAD(5)); S_(KLOAD(6)); S_(KLOAD(7)); S_(KLOAD(8)); S_(KLOAD(9));
    if (w < 216) {
      spin_lds(SY + SY_QP, 1);
      S_(W1LOAD(0)); S_(W1LOAD(1)); S_(W1LOAD(2));
      S_(KLOAD(10)); S_(KLOAD(11)); S_(KLOAD(12)); S_(KLOAD(13));
    } else {
      slot_k += 3;
      S_(KLOAD(10)); S_(KLOAD(11)); S_(KLOAD(12)); S_(KLOAD(13));
    }
    S_(KLOAD(14)); S_(KLOAD(15));
    S_(VLOAD(0)); S_(VLOAD(1));
    WAITV(8);
    lds_arrive(SY + SY_W1, lane);
    S_(VLOAD(2)); S_(VLOAD(3));
    S_(VLOAD(4)); S_(VLOAD(5)); S_(VLOAD(6)); S_(VLOAD(7));
    WAITV(8);
    spin_lds(SY + SY_Q, 1);
    f32x4 d[4];
#pragma unroll
    for (int t = 0; t < 4; ++t) d[t] = f32x4{0, 0, 0, 0};
#define QK(u) do { const v4u a_ = *(LAS v4u*)(L + L_QS + (c & 1) * 256 + (u) * 64 + g * 16); \
      _Pragma("unroll") for (int t = 0; t < 4; ++t) d[t] = mfma32v(a_, *(LAS v4u*)(L + L_K + j * 16384 + c * 1024 + t * 256 + (((4 * (u) + g) ^ c) * 16)), d[t]); \
      asm volatile("" : "+v"(d[0]), "+v"(d[1]), "+v"(d[2]), "+v"(d[3])); SB(); } while (0)
    S_(VLOAD(8)); QK(0); QK(1);
    S_(VLOAD(9)); QK(2); QK(3);
    S_(VLOAD(10));
    const float s0 = (g == 0 ? d[0][0] : g == 1 ? d[1][0] : g == 2 ? d[2][0] : d[3][0]) * C2;
    const float s1 = (g == 0 ? d[0][1] : g == 1 ? d[1][1] : g == 2 ? d[2][1] : d[3][1]) * C2;
    float m0 = wave_max(s0), m1 = wave_max(s1);
    asm volatile("" : "+v"(m0), "+v"(m1)); SB();
    S_(VLOAD(11));
    float p0 = __builtin_amdgcn_exp2f(s0 - m0), p1 = __builtin_amdgcn_exp2f(s1 - m1);
    const float l0 = wave_sum(p0), l1 = wave_sum(p1);
    if (lane == 0) *(LAS f32x4*)(L + L_WST + j * 16) = f32x4{m0, l0, m1, l1};
    lds_arrive(SY + SY_C0, lane);
    asm volatile("" : "+v"(p0), "+v"(p1)); SB();
    f32x2 acc[2][4];
#pragma unroll
    for (int k = 0; k < 4; ++k) { acc[0][k] = f32x2{0.f, 0.f}; acc[1][k] = f32x2{0.f, 0.f}; }
    const u16* wop = wo + (size_t)(64 * rho) * D + 144 * cb;
    const u16* w2p = w2 + (size_t)(72 * rho) * D + 144 * cb - 64 * D;
#define WLOAD(t) do { const int u_ = min((j + 8 * (t)) * 64 + lane, 136 * 18 - 1), r_ = u_ / 18; \
      Wt[t] = __builtin_nontemporal_load((const GAS v4u*)((r_ < 64 ? wop : w2p) + r_ * D + (u_ - r_ * 18) * 8)); } while (0)
#define PV(n) do { const float a_ = dpp<0x150 + (n)>(p0), b_ = dpp<0x150 + (n)>(p1); \
      _Pragma("unroll") for (int k = 0; k < 4; ++k) { const f32x2 v_ = {asf(V[n][k] << 16), asf(V[n][k] & 0xFFFF0000u)}; \
        acc[0][k] = __builtin_elementwise_fma(v_, f32x2{a_, a_}, acc[0][k]); acc[1][k] = __builtin_elementwise_fma(v_, f32x2{b_, b_}, acc[1][k]); } \
      asm volatile("" : "+v"(acc[0][0]), "+v"(acc[0][1]), "+v"(acc[0][2]), "+v"(acc[0][3]), "+v"(acc[1][0]), "+v"(acc[1][1]), "+v"(acc[1][2]), "+v"(acc[1][3])); SB(); } while (0)
    S_(VLOAD(12)); PV(0); PV(1); PV(2); PV(3);
    S_(VLOAD(13)); PV(4); PV(5); PV(6); PV(7);
    S_(VLOAD(14)); PV(8); PV(9); PV(10);
    S_(VLOAD(15)); PV(11);
    S_(WLOAD(0)); PV(12);
    S_(WLOAD(1)); PV(13);
    S_(WLOAD(2)); PV(14);
    spin_lds(SY + SY_G, 1);
    const float gf = *(LAS float*)(L + L_GF + j * 8 + (g >> 1) * 4);
    SB();
    S_(WLOAD(3)); WLOAD(4); SB(); PV(15);
    {
      float y[8], z[4];
#pragma unroll
      for (int k = 0; k < 8; ++k) { auto t = __builtin_amdgcn_permlane32_swap(asu(acc[0][k >> 1][k & 1]), asu(acc[1][k >> 1][k & 1]), false, false); y[k] = asf(t[0]) + asf(t[1]); }
#pragma unroll
      for (int k = 0; k < 4; ++k) { auto t = __builtin_amdgcn_permlane16_swap(asu(y[k]), asu(y[k + 4]), false, false); z[k] = asf(t[0]) + asf(t[1]); }
      *(LAS f32x4*)(L + L_RED + j * 1024 + (g >> 1) * 512 + c * 32 + (g & 1) * 16) = f32x4{z[0] * gf, z[1] * gf, z[2] * gf, z[3] * gf};
    }
    lds_arrive(SY + SY_C1, lane);
    const int kvc = rho >> 2, qt = rho & 3;
    const u32 tl = (u32)__builtin_amdgcn_readfirstlane((int)*(LAS u32*)(L + L_G + 8)), th = (u32)__builtin_amdgcn_readfirstlane((int)*(LAS u32*)(L + L_G + 12));
    const int h2off = (int)(H2_OFF + ((kvc * 16 + (j & 1) * 8 + (lane >> 3)) * 256 + 64 * qt + 16 * (j >> 1) + 2 * (lane & 7)) * 8);
    const int stoff = (int)(ST_OFF + ((kvc * 16 + (j & 1) * 8 + (lane >> 3)) * 4 + (qt >> 1) * 2) * 8);
    v4u st = ldg(rs, stoff);
    SB();
#pragma unroll
    for (int t = 0; t < 5; ++t) {
      const int u = (j + 8 * t) * 64 + lane;
      if (u < 136 * 18) *(LAS v4u*)(L + L_WI + u * 16) = Wt[t];
    }
    lds_arrive(SY + SY_C2, lane);
    spin_lds(SY + SY_C2, 8);
    spin_lds(SY + SY_H, 1);
    s16x4 bo[2][4];
    f32x4 dm[2];
    {
      LAS unsigned char* wb = L + L_WI + (4 * g + (c >> 2)) * 288 + (c & 3) * 8;
      LAS unsigned char* ha = L + L_HC + (lane & 1) * 160 + g * 8;
#pragma unroll
      for (int t = 0; t < 2; ++t) {
        if (t == 0 || j == 0) {
          LAS unsigned char* wt = wb + (t == 0 ? j * 32 : 256);
#pragma unroll
          for (int k = 0; k < 4; ++k) bo[t][k] = trd(wt + k * 16 * 288);
          dm[t] = f32x4{0, 0, 0, 0};
#pragma unroll
          for (int k = 0; k < 4; ++k) dm[t] = mfma16(*(LAS s16x4*)(ha + k * 32), trd(wt + (64 + k * 16) * 288), dm[t]);
          dm[t] = mfma16(*(LAS s16x4*)(ha + 128), trd(wt + (64 + 56) * 288), dm[t]);
        }
      }
    }
    const float g0 = *(LAS float*)(L + L_G), g1 = *(LAS float*)(L + L_G + 4);
#pragma nounroll
    for (u32 it = 0; it < CAP_GLB && !__all(g_bad(st, tl, th) == 0u); ++it) { st = ldg(rs, stoff); MEMBAR(); }
    float fs;
    {
      const float mv = g_val(st.x);
      const float M = max16_32(fmaxf(mv, dpp<ROR8>(mv)));
      const float e = __builtin_amdgcn_exp2f(mv - M);
      float l = e * g_val(st.z); l = sum16_32(l + dpp<ROR8>(l));
      fs = e / l;
      asm volatile("" : "+v"(fs));
    }
    v4u o = ldg(rs, h2off);
#pragma nounroll
    for (u32 it = 0; it < CAP_GLB && !__all(g_bad(o, tl, th) == 0u); ++it) { o = ldg(rs, h2off); MEMBAR(); }
    {
      float a0 = fs * g_val(o.x), a1 = fs * g_val(o.z);
      a0 += dpp<ROR8>(a0); a1 += dpp<ROR8>(a1); a0 = sum16_32(a0); a1 = sum16_32(a1);
      if (lane < 8) *(LAS u32*)(L + L_OC + (j & 1) * 128 + (16 * (j >> 1) + 2 * lane) * 2) = pk_bf16(a0, a1);
    }
    lds_arrive(SY + SY_C3, lane);
    spin_lds_tight(SY + SY_C3, 8);
    {
      LAS unsigned char* oa = L + L_OC + (lane & 1) * 128 + g * 8;
      s16x4 ao[4];
#pragma unroll
      for (int k = 0; k < 4; ++k) ao[k] = *(LAS s16x4*)(oa + k * 32);
#pragma unroll
      for (int t = 0; t < 2; ++t) {
        if (t == 0 || j == 0) {
          f32x4 da = {0, 0, 0, 0};
#pragma unroll
          for (int k = 0; k < 4; ++k) da = mfma16(ao[k], bo[t][k], da);
          const float v0 = g0 * da[0] + (1.f - g0) * dm[t][0], v1 = g1 * da[1] + (1.f - g1) * dm[t][1];
          const int col = 144 * cb + (t == 0 ? 16 * j : 128) + c;
          if (lane < 16) stg(g_pack(v0, v1, tl, th), rs, H3_OFF + (((col >> 2) * 64 + rho) * 4 + (col & 3)) * 16);
        }
      }
    }
  }
}

extern "C" void kernel_launch(void* const* d_in, const int* in_sizes, int n_in, void* d_out, int out_size, void* d_ws, size_t ws_size, hipStream_t stream) {
  (void)in_sizes; (void)n_in; (void)out_size; (void)ws_size;
  (void)hipFuncSetAttribute((const void*)fused_decode, hipFuncAttributeMaxDynamicSharedMemorySize, L_END);
  fused_decode<<<NWG, NTHR, L_END, stream>>>((const u16*)d_in[0], (const u16*)d_in[1], (const u16*)d_in[2], (const u16*)d_in[3], (const u16*)d_in[4],
      (const u16*)d_in[6], (const u16*)d_in[7], (const u16*)d_in[8], (const u16*)d_in[9], (const u16*)d_in[10], (u16*)d_out, (unsigned char*)d_ws);
}
```
